```python
import torch

def _softmax(x, axis=-1):
    return torch.softmax(x, dim=axis)
def _gelu(x):
    return torch.nn.functional.gelu(x, approximate="tanh")
def _silu(x):
    return torch.nn.functional.silu(x)
def _sigmoid(x):
    return torch.sigmoid(x)
def _layernorm(x, axis=-1, eps=1e-5):
    m = x.mean(dim=axis, keepdim=True)
    v = x.var(dim=axis, keepdim=True, unbiased=False)
    return (x - m) / torch.sqrt(v + eps)
def _scatter_add(tgt, idx, src, axis=0):
    return tgt.clone().index_add_(axis, idx, src)
def _segment_sum(src, seg, n):
    out = torch.zeros((n,) + src.shape[1:], dtype=src.dtype)
    return out.index_add_(0, seg, src)
def _pad(x, pw):
    flat = tuple(v for pair in reversed(pw) for v in pair)
    return torch.nn.functional.pad(x, flat)

kernel_name = 'gated_mix__ssm_conv_h3__mixer_channel_mlp__prompt'

def setup_inputs():

    _w = (lambda a: (lambda t: t.to(torch.bfloat16) if t.is_floating_point() else t)(torch.as_tensor(a)))
    def _A_setup():
        B, S, D = 1, 2048, 640
        ks = 4
        return {
            "q": _w((lambda *s: torch.randn(*s, dtype=torch.bfloat16) * 0.05)(B, S, D)),
            "k": _w((lambda *s: torch.randn(*s, dtype=torch.bfloat16) * 0.4)(B, S, D)),
            "v": _w((lambda *s: torch.randn(*s, dtype=torch.bfloat16) * 0.4)(B, S, D)),
            "shift_k": _w((lambda *s: torch.randn(*s, dtype=torch.bfloat16) * 0.4)(D, ks) * 0.5),
            "ssm_k": _w((lambda *s: torch.randn(*s, dtype=torch.bfloat16) * 1.6)(D, S) * (1.0 / S ** 0.5)),
            "ssm_D": _w((lambda *s: torch.randn(*s, dtype=torch.bfloat16) * 0.4)(D)),
        }
    _A_in = _A_setup()

    def _B_setup():
        B, S, D, F = 1, 2048, 640, 1536
        return {
            "x": _w((lambda *s: torch.randn(*s, dtype=torch.bfloat16) * 0.05)(B, S, D)),
            "w1": _w((lambda *s: torch.randn(*s, dtype=torch.bfloat16) * 0.8)(D, F) * (D ** -0.5)),
            "b1": _w((lambda *s: torch.randn(*s, dtype=torch.bfloat16) * 0.4)(F)),
            "w2": _w((lambda *s: torch.randn(*s, dtype=torch.bfloat16) * 0.05)(F, D) * (F ** -0.5)),
            "b2": _w((lambda *s: torch.randn(*s, dtype=torch.bfloat16) * 0.05)(D)),
        }
    _B_in = _B_setup()

    return {
        "A_q": _A_in["q"],
        "A_k": _A_in["k"],
        "A_v": _A_in["v"],
        "A_shift_k": _A_in["shift_k"],
        "A_ssm_k": _A_in["ssm_k"],
        "A_ssm_D": _A_in["ssm_D"],
        "B_x": _B_in["x"],
        "B_w1": _B_in["w1"],
        "B_b1": _B_in["b1"],
        "B_w2": _B_in["w2"],
        "B_b2": _B_in["b2"],
        "gate_w": _w((lambda *s: torch.randn(*s, dtype=torch.bfloat16) * 0.05)(640) * 16.0),
    }

def reference(A_q, A_k, A_v, A_shift_k, A_ssm_k, A_ssm_D, B_x, B_w1, B_b1, B_w2, B_b2, gate_w):
    def _A_ref(q, k, v, shift_k, ssm_k, ssm_D):
        B, S, D = q.shape
        ks = shift_k.shape[-1]
        k_pad = _pad(k, ((0, 0), (ks - 1, 0), (0, 0)))
        k_shift = torch.zeros_like(k)
        for t in range(ks):
            k_shift = k_shift + k_pad[:, t:t + S, :] * shift_k[:, ks - 1 - t][None, None, :]
        kv = k_shift * v
        L = 2 * S
        kv_pad = _pad(kv, ((0, 0), (0, S), (0, 0)))
        kf_pad = _pad(ssm_k, ((0, 0), (0, S)))
        kv_f = (lambda x, axes=(-2,-1): torch.fft.rfft2(x.float(), dim=axes))(kv_pad[:, None, :, :], axes=(1, 2))
        kf_f = (lambda x, axes=(-2,-1): torch.fft.rfft2(x.float(), dim=axes))(kf_pad[:, None, :], axes=(1, 2))
        kf_f = (lambda x, *d: x.permute(*d))(kf_f, 1, 2, 0)
        y_f = kv_f * kf_f[None, :, :, :]
        y = (lambda x, s, axes=(-2,-1): torch.fft.irfft2(x, s=s, dim=axes).to(torch.bfloat16))(y_f, (1, L), axes=(1, 2))[:, 0, :S, :]
        y = y + kv * ssm_D[None, None, :]
        return q * y

    def _B_ref(x, w1, b1, w2, b2):
        y = _layernorm(x, axis=-1)
        h = _gelu(torch.einsum("bsd,df->bsf", y, w1) + b1)
        return torch.einsum("bsf,fd->bsd", h, w2) + b2

    x = A_q
    g = _sigmoid((x * gate_w).sum(-1, keepdims=True))
    a = _A_ref(x, A_k, A_v, A_shift_k, A_ssm_k, A_ssm_D)
    b = _B_ref(x, B_w1, B_b1, B_w2, B_b2)
    return g * a + (1 - g) * b
```

```cpp
#include <hip/hip_runtime.h>
#include <stdint.h>

#define GAS __attribute__((address_space(1)))
#define LAS __attribute__((address_space(3)))
#define DI __device__ __forceinline__

typedef unsigned v4u __attribute__((ext_vector_type(4)));
typedef unsigned v2u __attribute__((ext_vector_type(2)));
typedef float f32x4 __attribute__((ext_vector_type(4)));
typedef float f32x2 __attribute__((ext_vector_type(2)));
typedef __bf16 bf16x8 __attribute__((ext_vector_type(8)));
typedef __bf16 bf16x2 __attribute__((ext_vector_type(2)));
typedef short s16x4 __attribute__((ext_vector_type(4)));
typedef short s16x8 __attribute__((ext_vector_type(8)));
typedef __amdgpu_buffer_rsrc_t rsrc_t;

constexpr int S_ = 2048, D_ = 640, F_ = 1536;
constexpr int AUX_SC1 = 16, AUX_NT = 2;

constexpr size_t WS_KVR = 65536;
constexpr size_t WS_H   = WS_KVR + (size_t)D_ * S_ * 2;
constexpr size_t WS_Y   = WS_H + (size_t)S_ * F_ * 2;
constexpr size_t WS_G   = WS_Y + (size_t)D_ * S_ * 2;
constexpr size_t WS_END = WS_G + S_ * 4;

DI f32x4 mfma16(v4u a, v4u b, f32x4 c) {
    return __builtin_amdgcn_mfma_f32_16x16x32_bf16(__builtin_bit_cast(bf16x8, a), __builtin_bit_cast(bf16x8, b), c, 0, 0, 0);
}
DI unsigned pk_bf16(float lo, float hi) {
    f32x2 v = {lo, hi};
    return __builtin_bit_cast(unsigned, __builtin_convertvector(v, bf16x2));
}
DI float bf_lo(unsigned u) { return __uint_as_float(u << 16); }
DI float bf_hi(unsigned u) { return __uint_as_float(u & 0xffff0000u); }
DI rsrc_t make_rsrc(const void* p, unsigned bytes) { return __builtin_amdgcn_make_buffer_rsrc((void*)p, 0, bytes, 0x00020000); }
DI v2u tr_read(LAS char* p) { return __builtin_bit_cast(v2u, __builtin_amdgcn_ds_read_tr16_b64_v4i16((LAS s16x4*)p)); }
DI v4u lds_ld128(LAS char* p) { return *(LAS v4u*)p; }
DI v2u lds_ld64(LAS char* p) { return *(LAS v2u*)p; }
DI float dot2bf(unsigned a, unsigned b, float c) {
    return __builtin_amdgcn_fdot2_f32_bf16(__builtin_bit_cast(bf16x2, a), __builtin_bit_cast(bf16x2, b), c, false);
}
DI void dma16(rsrc_t rs, unsigned ldsaddr, unsigned voff, unsigned soff) {
    asm volatile("s_mov_b32 m0, %0\n\ts_nop 0\n\tbuffer_load_dwordx4 %1, %2, %3 offen lds" :: "s"(ldsaddr), "v"(voff), "s"(rs), "s"(soff) : "memory");
}
template <int N> DI void wait_vm() { asm volatile("s_waitcnt vmcnt(%0)" :: "i"(N) : "memory"); }
DI void wg_barrier() { asm volatile("s_waitcnt lgkmcnt(0)\n\ts_barrier" ::: "memory"); }

struct Params {
    const unsigned short *q, *k, *v, *shift, *ssm, *ssmD, *w1, *b1, *w2, *b2, *gw;
    unsigned short* out;
    char* ws;
};

DI void prepass_task(const Params& p, int task, int lane) {
    const int cht = task % 10, tt = task / 10;
    const int cg = lane & 15, tg = lane >> 4;
    const int ch = 64 * cht + 4 * cg, tb = 32 * tt + 8 * tg;
    rsrc_t rk = make_rsrc(p.k, S_ * D_ * 2), rv = make_rsrc(p.v, S_ * D_ * 2), rw = make_rsrc(p.ws, (unsigned)WS_END);
    const unsigned off0 = tb * (D_ * 2) + ch * 2;
    const unsigned offh = tb > 0 ? off0 - 3 * (D_ * 2) : 0x80000000u;
    v2u kk[11], vv[8];
#pragma unroll
    for (int i = 0; i < 3; ++i) kk[i] = __builtin_amdgcn_raw_buffer_load_b64(rk, offh + i * (D_ * 2), 0, AUX_NT);
#pragma unroll
    for (int i = 0; i < 8; ++i) kk[3 + i] = __builtin_amdgcn_raw_buffer_load_b64(rk, off0 + i * (D_ * 2), 0, AUX_NT);
#pragma unroll
    for (int i = 0; i < 8; ++i) vv[i] = __builtin_amdgcn_raw_buffer_load_b64(rv, off0 + i * (D_ * 2), 0, AUX_NT);
    const v4u sA = *(const GAS v4u*)(p.shift + ch * 4), sB = *(const GAS v4u*)(p.shift + ch * 4 + 8);
    float s[4][4] = {{bf_lo(sA.x), bf_hi(sA.x), bf_lo(sA.y), bf_hi(sA.y)}, {bf_lo(sA.z), bf_hi(sA.z), bf_lo(sA.w), bf_hi(sA.w)},
                     {bf_lo(sB.x), bf_hi(sB.x), bf_lo(sB.y), bf_hi(sB.y)}, {bf_lo(sB.z), bf_hi(sB.z), bf_lo(sB.w), bf_hi(sB.w)}};
    float kf[11][4];
#pragma unroll
    for (int i = 0; i < 11; ++i) { kf[i][0] = bf_lo(kk[i].x); kf[i][1] = bf_hi(kk[i].x); kf[i][2] = bf_lo(kk[i].y); kf[i][3] = bf_hi(kk[i].y); }
    float kv[8][4];
#pragma unroll
    for (int t = 0; t < 8; ++t) {
        const float vf[4] = {bf_lo(vv[t].x), bf_hi(vv[t].x), bf_lo(vv[t].y), bf_hi(vv[t].y)};
#pragma unroll
        for (int c = 0; c < 4; ++c) {
            float a = s[c][0] * kf[t + 3][c];
            a = fmaf(s[c][1], kf[t + 2][c], a); a = fmaf(s[c][2], kf[t + 1][c], a); a = fmaf(s[c][3], kf[t][c], a);
            kv[t][c] = a * vf[c];
        }
    }
#pragma unroll
    for (int c = 0; c < 4; ++c) {
        v4u o = {pk_bf16(kv[7][c], kv[6][c]), pk_bf16(kv[5][c], kv[4][c]), pk_bf16(kv[3][c], kv[2][c]), pk_bf16(kv[1][c], kv[0][c])};
        __builtin_amdgcn_raw_buffer_store_b128(o, rw, (unsigned)WS_KVR + (ch + c) * (S_ * 2) + (2040 - tb) * 2, 0, AUX_SC1);
    }
}
DI int task_of(int b, int w) { return b < 128 ? 3 * b + w : 384 + 2 * (b - 128) + w; }

__global__ void __launch_bounds__(256) k_pre(Params p) {
    const int w = __builtin_amdgcn_readfirstlane(threadIdx.x >> 6), lane = threadIdx.x & 63, b = blockIdx.x;
    if (w < (b < 128 ? 3 : 2)) prepass_task(p, task_of(b, w), lane);
}

constexpr int CV_COPY = 4160, CV_XR = 4 * CV_COPY, CV_REG = 21248;
DI void conv_build_h(const Params& p, int d, LAS char* reg, int lane) {
    rsrc_t rh = make_rsrc(p.ssm, S_ * D_ * 2);
    v4u w[4], wn[4];
#pragma unroll
    for (int i = 0; i < 4; ++i) {
        w[i] = __builtin_amdgcn_raw_buffer_load_b128(rh, d * 4096 + 1024 * i + 16 * lane, 0, 0);
        wn[i] = __builtin_amdgcn_raw_buffer_load_b128(rh, d * 4096 + 1024 * i + 16 * lane + 16, 0, 0);
    }
    const v2u h0 = __builtin_amdgcn_raw_buffer_load_b64(rh, d * 4096, 0, 0);
#pragma unroll
    for (int i = 0; i < 4; ++i) {
        const unsigned a[8] = {w[i].x, w[i].y, w[i].z, w[i].w, wn[i].x, wn[i].y, wn[i].z, wn[i].w};
        LAS char* dst = reg + 32 + 1024 * i + 16 * lane;
        *(LAS v4u*)(dst) = w[i];
        *(LAS v4u*)(dst + CV_COPY) = (v4u){__builtin_amdgcn_alignbit(a[1], a[0], 16), __builtin_amdgcn_alignbit(a[2], a[1], 16), __builtin_amdgcn_alignbit(a[3], a[2], 16), __builtin_amdgcn_alignbit(a[4], a[3], 16)};
        *(LAS v4u*)(dst + 2 * CV_COPY) = (v4u){a[1], a[2], a[3], a[4]};
        *(LAS v4u*)(dst + 3 * CV_COPY) = (v4u){__builtin_amdgcn_alignbit(a[2], a[1], 16), __builtin_amdgcn_alignbit(a[3], a[2], 16), __builtin_amdgcn_alignbit(a[4], a[3], 16), __builtin_amdgcn_alignbit(a[5], a[4], 16)};
    }
    if (lane < 8) {
        const int rho = lane >> 1;
        v4u z = {0, 0, 0, 0};
        if (lane & 1) {
            if (rho == 1) z.w = h0.x << 16;
            if (rho == 2) z.w = h0.x;
            if (rho == 3) { z.z = h0.x << 16; z.w = __builtin_amdgcn_alignbit(h0.y, h0.x, 16); }
        }
        *(LAS v4u*)(reg + rho * CV_COPY + 16 * (lane & 1)) = z;
    }
}
template <bool SC1>
DI void conv_load_x(const Params& p, int d, LAS char* reg, int lane) {
    rsrc_t rw = make_rsrc(p.ws, (unsigned)WS_END);
    v4u x[4];
#pragma unroll
    for (int i = 0; i < 4; ++i) x[i] = __builtin_amdgcn_raw_buffer_load_b128(rw, (unsigned)WS_KVR + d * 4096 + 1024 * i + 16 * lane, 0, SC1 ? AUX_SC1 : 0);
#pragma unroll
    for (int i = 0; i < 4; ++i) *(LAS v4u*)(reg + CV_XR + 1024 * i + 16 * lane) = x[i];
    if (lane < 30) *(LAS v4u*)(reg + CV_XR + 4096 + 16 * lane) = (v4u){0, 0, 0, 0};
}
DI void conv_compute(const Params& p, int d, LAS char* reg, int lane) {
    const int i = lane & 15, kq = lane >> 4;
    const int pp = 1 + i + 8 * kq;
    const int rho = pp & 3;
    LAS char* abase = reg + rho * CV_COPY + 2 * (pp - rho);
    LAS char* bbase = reg + CV_XR + 4064 - 32 * i + 16 * kq - 3584;
    f32x4 acc[8];
    {
        const unsigned dv = p.ssmD[d];
        const int jj = 15 - i - 8 * kq;
        const unsigned val = (jj & 1) ? (dv << 16) : dv;
        v4u ad;
        ad.x = (jj >> 1) == 0 ? val : 0; ad.y = (jj >> 1) == 1 ? val : 0; ad.z = (jj >> 1) == 2 ? val : 0; ad.w = (jj >> 1) == 3 ? val : 0;
        if (jj < 0) ad = (v4u){0, 0, 0, 0};
#pragma unroll
        for (int c = 0; c < 8; ++c) {
            const v4u bb = lds_ld128(bbase + 512 * (7 - c));
            acc[c] = mfma16(ad, bb, (f32x4){0.f, 0.f, 0.f, 0.f});
        }
    }
#pragma unroll
    for (int dl = 0; dl < 8; ++dl) {
        v4u A[8], B[8];
#pragma unroll
        for (int m = 0; m < 8; ++m) {
            const v2u lo = lds_ld64(abase + 64 * dl + 512 * m), hi = lds_ld64(abase + 64 * dl + 512 * m + 8);
            A[m] = (v4u){lo.x, lo.y, hi.x, hi.y};
            B[m] = lds_ld128(bbase + 512 * (7 - m) + 64 * dl);
        }
#pragma unroll
        for (int c = 0; c < 8; ++c)
#pragma unroll
            for (int m = 0; m <= c; ++m) acc[c] = mfma16(A[m], B[c - m], acc[c]);
    }
    rsrc_t rw = make_rsrc(p.ws, (unsigned)WS_END);
    const int g = kq, j = i;
#pragma unroll
    for (int c = 0; c < 8; ++c) {
        v2u o = {pk_bf16(acc[c][0], acc[c][1]), pk_bf16(acc[c][2], acc[c][3])};
        __builtin_amdgcn_raw_buffer_store_b64(o, rw, (unsigned)WS_Y + (d * S_ + 256 * c + 16 * j + 4 * g) * 2, 0, AUX_SC1);
    }
}
__global__ void __launch_bounds__(256) k_conv(Params p) {
    extern __shared__ __attribute__((aligned(256))) char lds_[];
    LAS char* lds = (LAS char*)lds_;
    const int w = __builtin_amdgcn_readfirstlane(threadIdx.x >> 6), lane = threadIdx.x & 63, b = blockIdx.x;
    if (w < (b < 128 ? 3 : 2)) {
        const int d = task_of(b, w);
        LAS char* reg = lds + w * CV_REG;
        conv_build_h(p, d, reg, lane);
        conv_load_x<false>(p, d, reg, lane);
        asm volatile("s_waitcnt lgkmcnt(0)" ::: "memory");
        conv_compute(p, d, reg, lane);
    }
}

constexpr int G1_SLOT = 32768, G1_W = 8192;
constexpr int L_R1 = 0;
constexpr int L_R2 = 98304;
constexpr int L_MISC = L_R2 + 3 * CV_REG;
constexpr int L_STATS = L_R1 + G1_SLOT;
constexpr int L_CS = L_STATS + 512;
constexpr int L_END = 163840;
DI float gelu_tanh(float u) {
    const float c0 = -2.0f * 0.7978845608028654f * 1.4426950408889634f, c1 = c0 * 0.044715f;
    const float z = u * fmaf(u * u, c1, c0);
    return u * __builtin_amdgcn_rcpf(1.0f + __builtin_amdgcn_exp2f(z));
}
DI void tile_of(int b, int& R, int& ct) { const int xcd = b & 7, r = b >> 3; R = 8 * (xcd >> 1) + (r >> 2); ct = 4 * (xcd & 1) + (r & 3); }

__global__ void __launch_bounds__(512, 2) k_g1(Params p) {
    extern __shared__ __attribute__((aligned(256))) char lds_[];
    LAS char* lds = (LAS char*)lds_;
    const unsigned ldsb = (unsigned)(uintptr_t)lds;
    const int w = __builtin_amdgcn_readfirstlane(threadIdx.x >> 6), lane = threadIdx.x & 63, b = blockIdx.x;
    int R, ct; tile_of(b, R, ct);
    LAS char* gate = lds + L_R2;
    if (w >= 4) {
        const int a = w - 4;
        rsrc_t rx = make_rsrc(p.q, S_ * D_ * 2), rw1 = make_rsrc(p.w1, D_ * F_ * 2);
        unsigned xoff[2], woff[6];
#pragma unroll
        for (int i = 0; i < 2; ++i) { const int row = 8 * (2 * a + i) + (lane >> 3), pp = lane & 7; xoff[i] = (64 * R + row) * (D_ * 2) + 16 * (pp ^ ((row >> 1) & 7)); }
#pragma unroll
        for (int i = 0; i < 6; ++i) { const int pos = 64 * i + lane, rl = pos / 24, cc = pos % 24, row = 16 * a + rl, blk = cc >> 1, aa = blk >> 2, bb = blk & 3;
            const int sw = ((row >> 1) & 1) | (((row >> 3) & 1) << 1);
            woff[i] = row * (F_ * 2) + 384 * ct + 16 * (8 * aa + 2 * (bb ^ sw) + (cc & 1)); }
        auto issue = [&](int c) {
            const unsigned slot = ldsb + L_R1 + (c % 3) * G1_SLOT;
#pragma unroll
            for (int i = 0; i < 2; ++i) dma16(rx, slot + (2 * a + i) * 1024, xoff[i], c * 128);
#pragma unroll
            for (int i = 0; i < 6; ++i) dma16(rw1, slot + G1_W + (6 * a + i) * 1024, woff[i], c * 64 * (F_ * 2));
        };
        issue(0); issue(1);
        if (a == 0) { for (int i = lane; i < 80; i += 64) *(LAS v4u*)(gate + 16 * i) = *(const GAS v4u*)(p.gw + 8 * i); }
        float s1 = 0.f, s2 = 0.f, sg = 0.f;
        const int j = lane & 15, kq = lane >> 4;
        const int xrow = 16 * a + j, fsw = (xrow >> 1) & 7;
        for (int c = 0; c < 10; ++c) {
            if (c < 9) wait_vm<8>(); else wait_vm<0>();
            wg_barrier();
            if (c + 2 < 10) issue(c + 2);
            LAS char* slot = lds + L_R1 + (c % 3) * G1_SLOT;
#pragma unroll
            for (int s = 0; s < 2; ++s) {
                const v4u x = lds_ld128(slot + xrow * 128 + 16 * ((4 * s + kq) ^ fsw));
                const v4u gw = lds_ld128(gate + c * 128 + s * 64 + kq * 16);
                s1 = dot2bf(x.x, 0x3f803f80u, s1); s1 = dot2bf(x.y, 0x3f803f80u, s1); s1 = dot2bf(x.z, 0x3f803f80u, s1); s1 = dot2bf(x.w, 0x3f803f80u, s1);
                s2 = dot2bf(x.x, x.x, s2); s2 = dot2bf(x.y, x.y, s2); s2 = dot2bf(x.z, x.z, s2); s2 = dot2bf(x.w, x.w, s2);
                sg = dot2bf(x.x, gw.x, sg); sg = dot2bf(x.y, gw.y, sg); sg = dot2bf(x.z, gw.z, sg); sg = dot2bf(x.w, gw.w, sg);
            }
        }
        s1 += __shfl_xor(s1, 16); s2 += __shfl_xor(s2, 16); sg += __shfl_xor(sg, 16);
        s1 += __shfl_xor(s1, 32); s2 += __shfl_xor(s2, 32); sg += __shfl_xor(sg, 32);
        const float mean = s1 * (1.0f / D_), var = fmaxf(s2 * (1.0f / D_) - mean * mean, 0.f), rstd = 1.0f / sqrtf(var + 1e-5f);
        if (kq == 0) {
            *(LAS f32x2*)(lds + L_STATS + 8 * xrow) = (f32x2){rstd, -mean * rstd};
            if (ct == 0) { rsrc_t rw = make_rsrc(p.ws, (unsigned)WS_END);
                __builtin_amdgcn_raw_buffer_store_b32(__float_as_uint(1.0f / (1.0f + __expf(-sg))), rw, (unsigned)WS_G + (64 * R + xrow) * 4, 0, AUX_SC1); }
        }
        wg_barrier();
    } else {
        const int m = w >> 1, n = w & 1;
        const int j = lane & 15, kq = lane >> 4, q = (lane & 15) >> 2, pq = lane & 3;
        const int fsw = (j >> 1) & 7;
        const int xA = (32 * m + j) * 128 + 16 * ((4 * m + kq) ^ fsw), xB = (32 * m + j) * 128 + 16 * ((4 * (1 - m) + kq) ^ fsw);
        const int sw = ((q >> 1) & 1) | ((kq & 1) << 1);
        int wA[6], wB[6];
#pragma unroll
        for (int z = 0; z < 6; ++z) { const int blk = 6 * n + z, aa = blk >> 2, bb = blk & 3;
            const int o = G1_W + (8 * kq + q) * 384 + 128 * aa + 32 * (bb ^ sw) + 8 * pq;
            wA[z] = o + m * 32 * 384; wB[z] = o + (1 - m) * 32 * 384; }
        f32x4 acc[6][2], cs[6];
#pragma unroll
        for (int z = 0; z < 6; ++z) { acc[z][0] = acc[z][1] = cs[z] = (f32x4){0.f, 0.f, 0.f, 0.f}; }
        const v4u ones = {0x3f803f80u, 0x3f803f80u, 0x3f803f80u, 0x3f803f80u};
        v2u b1v[6];
#pragma unroll
        for (int z = 0; z < 6; ++z) b1v[z] = *(const GAS v2u*)(p.b1 + 192 * ct + 96 * n + 16 * z + 4 * kq);
        for (int c = 0; c < 10; ++c) {
            wg_barrier();
            LAS char* slot = lds + L_R1 + (c % 3) * G1_SLOT;
            {
                const v4u x0 = lds_ld128(slot + xA), x1 = lds_ld128(slot + xA + 2048);
#pragma unroll
                for (int z = 0; z < 6; ++z) {
                    const v2u lo = tr_read(slot + wA[z]), hi = tr_read(slot + wA[z] + 4 * 384);
                    const v4u wf = {lo.x, lo.y, hi.x, hi.y};
                    acc[z][0] = mfma16(wf, x0, acc[z][0]); acc[z][1] = mfma16(wf, x1, acc[z][1]); cs[z] = mfma16(wf, ones, cs[z]);
                }
            }
            {
                const v4u x0 = lds_ld128(slot + xB), x1 = lds_ld128(slot + xB + 2048);
#pragma unroll
                for (int z = 0; z < 6; ++z) {
                    const v2u lo = tr_read(slot + wB[z]), hi = tr_read(slot + wB[z] + 4 * 384);
                    const v4u wf = {lo.x, lo.y, hi.x, hi.y};
                    acc[z][0] = mfma16(wf, x0, acc[z][0]); acc[z][1] = mfma16(wf, x1, acc[z][1]);
                }
            }
        }
        if (j == 0) {
#pragma unroll
            for (int z = 0; z < 6; ++z) *(LAS f32x4*)(lds + L_CS + ((m * 2 + n) * 6 + z) * 64 + 16 * kq) = cs[z];
        }
        wg_barrier();
        rsrc_t rw = make_rsrc(p.ws, (unsigned)WS_END);
#pragma unroll
        for (int z = 0; z < 6; ++z) cs[z] += *(LAS f32x4*)(lds + L_CS + (((1 - m) * 2 + n) * 6 + z) * 64 + 16 * kq);
#pragma unroll
        for (int T = 0; T < 2; ++T) {
            const f32x2 st = *(LAS f32x2*)(lds + L_STATS + 8 * (32 * m + 16 * T + j));
#pragma unroll
            for (int zp = 0; zp < 3; ++zp) {
                unsigned o[4];
#pragma unroll
                for (int hz = 0; hz < 2; ++hz) {
                    const int z = 2 * zp + hz;
                    const float bb[4] = {bf_lo(b1v[z].x), bf_hi(b1v[z].x), bf_lo(b1v[z].y), bf_hi(b1v[z].y)};
                    float u[4];
#pragma unroll
                    for (int r = 0; r < 4; ++r) u[r] = gelu_tanh(fmaf(st.x, acc[z][T][r], fmaf(st.y, cs[z][r], bb[r])));
                    o[2 * hz] = pk_bf16(u[0], u[1]); o[2 * hz + 1] = pk_bf16(u[2], u[3]);
                }
                __builtin_amdgcn_raw_buffer_store_b128((v4u){o[0], o[1], o[2], o[3]}, rw,
                    (unsigned)WS_H + ((4 * R + 2 * m + T) * 48 + 6 * ct + 3 * n + zp) * 1024 + lane * 16, 0, AUX_SC1);
            }
        }
    }
}

constexpr int G2_NS = 4, G2_SLOT = 5120, G2_RING = G2_NS * G2_SLOT;
constexpr int G2_HD = 3;
DI void ld_h(v4u& dst, rsrc_t rs, unsigned voff, unsigned soff) {
    asm volatile("buffer_load_dwordx4 %0, %1, %2, %3 offen sc1" : "=v"(dst) : "v"(voff), "s"(rs), "s"(soff) : "memory");
}
template <int N> DI void wait_vm4(v4u& a, v4u& b, v4u& c, v4u& d) { asm volatile("s_waitcnt vmcnt(%4)" : "+v"(a), "+v"(b), "+v"(c), "+v"(d) : "i"(N) : "memory"); }

struct G2Ctx {
    rsrc_t rw2, rh; unsigned woff[5], hoff, ring; LAS char* ringp; int trb;
    f32x4 acc[5][4]; v4u h[G2_HD][4];
};
template <int S> DI void g2_issue_w(G2Ctx& c) {
#pragma unroll
    for (int i = 0; i < 5; ++i) dma16(c.rw2, c.ring + (S % G2_NS) * G2_SLOT + i * 1024, c.woff[i], S * 32 * (D_ * 2));
}
template <int S> DI void g2_issue_h(G2Ctx& c) {
#pragma unroll
    for (int T = 0; T < 4; ++T) ld_h(c.h[S % G2_HD][T], c.rh, c.hoff, T * 49152 + S * 1024);
}
constexpr int g2_after(int s) {
    int n = 0;
    if (s < G2_HD) { n += 4 * (G2_HD - 1 - s); for (int e = 0; e < s; ++e) n += (e + G2_NS < 12 ? 5 : 0) + (e + G2_HD < 12 ? 4 : 0); }
    else { for (int e = s - G2_HD + 1; e < s; ++e) n += (e + G2_NS < 12 ? 5 : 0) + (e + G2_HD < 12 ? 4 : 0); }
    return n;
}
template <int S> DI void g2_step(G2Ctx& c) {
    wait_vm4<g2_after(S)>(c.h[S % G2_HD][0], c.h[S % G2_HD][1], c.h[S % G2_HD][2], c.h[S % G2_HD][3]);
    v4u wf[5];
    LAS char* sl = c.ringp + (S % G2_NS) * G2_SLOT + c.trb;
#pragma unroll
    for (int t5 = 0; t5 < 5; ++t5) { const v2u lo = tr_read(sl + 32 * t5), hi = tr_read(sl + 32 * t5 + 16 * 160); wf[t5] = (v4u){lo.x, lo.y, hi.x, hi.y}; }
#pragma unroll
    for (int t5 = 0; t5 < 5; ++t5)
#pragma unroll
        for (int T = 0; T < 4; ++T) c.acc[t5][T] = mfma16(wf[t5], c.h[S % G2_HD][T], c.acc[t5][T]);
    asm volatile("s_waitcnt lgkmcnt(0)" ::: "memory");
    __builtin_amdgcn_sched_barrier(0);
    if constexpr (S + G2_NS < 12) g2_issue_w<S + G2_NS>(c);
    if constexpr (S + G2_HD < 12) g2_issue_h<S + G2_HD>(c);
    __builtin_amdgcn_sched_barrier(0);
}
template <int... I> struct iseq {};
template <int N, int... I> struct mkseq : mkseq<N - 1, N - 1, I...> {};
template <int... I> struct mkseq<0, I...> { typedef iseq<I...> type; };
template <int... I> DI void g2_steps(G2Ctx& c, iseq<I...>) { (g2_step<I>(c), ...); }

__global__ void __launch_bounds__(256) k_g2(Params p) {
    extern __shared__ __attribute__((aligned(256))) char lds_[];
    LAS char* lds = (LAS char*)lds_;
    const unsigned ldsb = (unsigned)(uintptr_t)lds;
    const int w = __builtin_amdgcn_readfirstlane(threadIdx.x >> 6), lane = threadIdx.x & 63, b = blockIdx.x;
    int R, nt; tile_of(b, R, nt);
    const int j = lane & 15, kq = lane >> 4, q = (lane & 15) >> 2, pq = lane & 3;
    G2Ctx c;
    c.rw2 = make_rsrc(p.w2 + (size_t)(384 * w) * D_, (unsigned)((F_ - 384 * w) * D_ * 2));
    c.rh = make_rsrc(p.ws + WS_H + ((size_t)(4 * R) * 48 + 12 * w) * 1024, 4 * 49152);
#pragma unroll
    for (int i = 0; i < 5; ++i) { const int pos = 64 * i + lane, row = pos / 10, cc = pos % 10; c.woff[i] = row * (D_ * 2) + 160 * nt + 16 * cc; }
    c.hoff = lane * 16;
    c.ring = ldsb + w * G2_RING; c.ringp = lds + w * G2_RING;
    c.trb = (4 * kq + q) * 160 + 8 * pq;
#pragma unroll
    for (int t5 = 0; t5 < 5; ++t5)
#pragma unroll
        for (int T = 0; T < 4; ++T) c.acc[t5][T] = (f32x4){0.f, 0.f, 0.f, 0.f};
    g2_issue_w<0>(c); g2_issue_w<1>(c); g2_issue_w<2>(c); g2_issue_w<3>(c);
    g2_issue_h<0>(c); g2_issue_h<1>(c); g2_issue_h<2>(c);
    g2_steps(c, mkseq<12>::type{});
    LAS char* red = lds + w * G2_RING;
#pragma unroll
    for (int T = 0; T < 4; ++T) {
        if (T != w) {
            const int slot = T < w ? T : T - 1;
#pragma unroll
            for (int t5 = 0; t5 < 5; ++t5) *(LAS f32x4*)(red + (slot * 5 + t5) * 1024 + lane * 16) = c.acc[t5][T];
        }
    }
    LAS char* ytile = lds + 4 * G2_RING;
    {
        rsrc_t rw = make_rsrc(p.ws, (unsigned)WS_END);
        for (int i = threadIdx.x; i < 640; i += 256) { const int ch = i >> 3, cc = i & 7;
            const v4u y = __builtin_amdgcn_raw_buffer_load_b128(rw, (unsigned)WS_Y + ((80 * nt + ch) * S_ + 64 * R) * 2 + 16 * cc, 0, AUX_SC1);
            *(LAS v4u*)(ytile + ch * 160 + 16 * cc) = y; }
    }
    wg_barrier();
    f32x4 fin[5];
#pragma unroll
    for (int t5 = 0; t5 < 5; ++t5) {
        f32x4 sacc = w == 0 ? c.acc[t5][0] : w == 1 ? c.acc[t5][1] : w == 2 ? c.acc[t5][2] : c.acc[t5][3];
#pragma unroll
        for (int o = 0; o < 4; ++o) {
            if (o != w) { const int slot = w < o ? w : w - 1; sacc += *(LAS f32x4*)(lds + o * G2_RING + (slot * 5 + t5) * 1024 + lane * 16); }
        }
        fin[t5] = sacc;
    }
    const int tok = 64 * R + 16 * w + j;
    const float g = __uint_as_float(__builtin_amdgcn_raw_buffer_load_b32(make_rsrc(p.ws, (unsigned)WS_END), (unsigned)WS_G + tok * 4, 0, AUX_SC1));
#pragma unroll
    for (int t5 = 0; t5 < 5; ++t5) {
        const int ch = 80 * nt + 16 * t5 + 4 * kq;
        const v2u qv = *(const GAS v2u*)(p.q + (size_t)tok * D_ + ch), bv = *(const GAS v2u*)(p.b2 + ch);
        const v2u yv = tr_read(ytile + (16 * t5 + 4 * kq + q) * 160 + 32 * w + 8 * pq);
        const float qf[4] = {bf_lo(qv.x), bf_hi(qv.x), bf_lo(qv.y), bf_hi(qv.y)}, yf[4] = {bf_lo(yv.x), bf_hi(yv.x), bf_lo(yv.y), bf_hi(yv.y)}, bf[4] = {bf_lo(bv.x), bf_hi(bv.x), bf_lo(bv.y), bf_hi(bv.y)};
        float o[4];
#pragma unroll
        for (int r = 0; r < 4; ++r) o[r] = fmaf(g, qf[r] * yf[r], (1.0f - g) * (fin[t5][r] + bf[r]));
        *(GAS v2u*)(p.out + (size_t)tok * D_ + ch) = (v2u){pk_bf16(o[0], o[1]), pk_bf16(o[2], o[3])};
    }
}

extern "C" void kernel_launch(void* const* d_in, const int* in_sizes, int n_in, void* d_out, int out_size, void* d_ws, size_t ws_size, hipStream_t stream) {
    Params p;
    p.q = (const unsigned short*)d_in[0]; p.k = (const unsigned short*)d_in[1]; p.v = (const unsigned short*)d_in[2];
    p.shift = (const unsigned short*)d_in[3]; p.ssm = (const unsigned short*)d_in[4]; p.ssmD = (const unsigned short*)d_in[5];
    p.w1 = (const unsigned short*)d_in[7]; p.b1 = (const unsigned short*)d_in[8]; p.w2 = (const unsigned short*)d_in[9]; p.b2 = (const unsigned short*)d_in[10];
    p.gw = (const unsigned short*)d_in[11];
    p.out = (unsigned short*)d_out; p.ws = (char*)d_ws;
    (void)hipFuncSetAttribute((const void*)k_g1, hipFuncAttributeMaxDynamicSharedMemorySize, L_END);
    (void)hipFuncSetAttribute((const void*)k_conv, hipFuncAttributeMaxDynamicSharedMemorySize, 3 * CV_REG);
    (void)hipFuncSetAttribute((const void*)k_g2, hipFuncAttributeMaxDynamicSharedMemorySize, 4 * G2_RING + 80 * 160);
    k_pre<<<256, 256, 0, stream>>>(p);
    k_g1<<<256, 512, L_END, stream>>>(p);
    k_conv<<<256, 256, 3 * CV_REG, stream>>>(p);
    k_g2<<<256, 256, 4 * G2_RING + 80 * 160, stream>>>(p);
}
```

```cpp
#include <hip/hip_runtime.h>
#include <stdint.h>

#define GAS __attribute__((address_space(1)))
#define LAS __attribute__((address_space(3)))
#define DI __device__ __forceinline__

typedef unsigned v4u __attribute__((ext_vector_type(4)));
typedef unsigned v2u __attribute__((ext_vector_type(2)));
typedef float f32x4 __attribute__((ext_vector_type(4)));
typedef float f32x2 __attribute__((ext_vector_type(2)));
typedef __bf16 bf16x8 __attribute__((ext_vector_type(8)));
typedef __bf16 bf16x2 __attribute__((ext_vector_type(2)));
typedef short s16x4 __attribute__((ext_vector_type(4)));
typedef unsigned long long u64;
typedef __amdgpu_buffer_rsrc_t rsrc_t;

constexpr int S_ = 2048, D_ = 640, F_ = 1536;
constexpr int AUX_SC1 = 16, AUX_NT = 2;
constexpr unsigned OOB = 0x80000000u;
constexpr unsigned BIAS = 4096;

constexpr unsigned WS_E    = 0;
constexpr unsigned WS_FKV  = 4096;
constexpr unsigned WS_FH   = WS_FKV + 640 * 8;
constexpr unsigned WS_FY   = WS_FH + 1024 * 8;
constexpr unsigned WS_KVR  = 65536;
constexpr unsigned WS_H    = WS_KVR + D_ * S_ * 2;
constexpr unsigned WS_Y    = WS_H + S_ * F_ * 2;
constexpr unsigned WS_END  = WS_Y + D_ * S_ * 2;

constexpr int L_X = 0, X_CH = 8192;
constexpr int L_W = 81920, W_SL = 12288, NW = 6;
constexpr int G2_NS = 4, G2_PIECE = 5120, G2_SLOT = 4 * G2_PIECE;
constexpr int CV_COPY = 4160, CV_XR = 4 * CV_COPY, CV_REG = 21248;
constexpr int L_CV = 81920;
constexpr int L_FIN3 = L_CV + 3 * CV_REG;
constexpr int L_MISC = L_W + NW * W_SL;
constexpr int L_GATE = L_MISC;
constexpr int L_STATS = L_GATE + 1280;
constexpr int L_CS = L_STATS + 512;
constexpr int L_OMG = L_CS + 768;
constexpr int L_FLAG = L_OMG + 256;
constexpr int L_DUMMY = L_FLAG + 128;
constexpr int L_END = 163840;
constexpr int F_LANDED = 0, F_DONE = 16, F_LANDED2 = 32, F_DONE2 = 48;
static_assert(L_FIN3 + 7680 <= L_MISC && L_DUMMY + 256 <= L_END && G2_NS * G2_SLOT <= L_CV, "lds map");

#define STAMP(idx) do {} while (0)
#define CSTAMP(idx) do {} while (0)

DI f32x4 mfma16(v4u a, v4u b, f32x4 c) {
    return __builtin_amdgcn_mfma_f32_16x16x32_bf16(__builtin_bit_cast(bf16x8, a), __builtin_bit_cast(bf16x8, b), c, 0, 0, 0);
}
DI unsigned pk_bf16(float lo, float hi) { f32x2 v = {lo, hi}; return __builtin_bit_cast(unsigned, __builtin_convertvector(v, bf16x2)); }
DI unsigned pk_bf16v(f32x2 v) { return __builtin_bit_cast(unsigned, __builtin_convertvector(v, bf16x2)); }
DI float bf_lo(unsigned u) { return __uint_as_float(u << 16); }
DI float bf_hi(unsigned u) { return __uint_as_float(u & 0xffff0000u); }
DI rsrc_t make_rsrc(const void* p, unsigned bytes) { return __builtin_amdgcn_make_buffer_rsrc((void*)p, 0, bytes, 0x00020000); }
DI rsrc_t make_rsrc_biased(const void* p, unsigned bytes) { return __builtin_amdgcn_make_buffer_rsrc((void*)((const char*)p - BIAS), 0, bytes + BIAS, 0x00020000); }
DI v2u tr_read(LAS char* p) { return __builtin_bit_cast(v2u, __builtin_amdgcn_ds_read_tr16_b64_v4i16((LAS s16x4*)p)); }
DI v4u tr_read2(LAS char* p, int d) { const v2u lo = tr_read(p), hi = tr_read(p + d); return (v4u){lo.x, lo.y, hi.x, hi.y}; }
DI v4u lds_ld128(LAS char* p) { return *(LAS v4u*)p; }
DI v2u lds_ld64v(LAS char* p) { return *(volatile LAS v2u*)p; }
DI float dot2bf(unsigned a, unsigned b, float c) { return __builtin_amdgcn_fdot2_f32_bf16(__builtin_bit_cast(bf16x2, a), __builtin_bit_cast(bf16x2, b), c, false); }
DI void dma1(rsrc_t rs, unsigned ldsaddr, unsigned voff, unsigned soff) {
    asm volatile("s_mov_b32 m0, %0\n\ts_nop 0\n\tbuffer_load_dwordx4 %1, %2, %3 offen lds" :: "s"(ldsaddr), "v"(voff), "s"(rs), "s"(soff) : "memory");
}
DI void dma4(rsrc_t rs, unsigned ldsaddr, unsigned v0, unsigned v1, unsigned v2, unsigned v3, unsigned soff) {
    asm volatile("s_mov_b32 m0, %0\n\ts_nop 0\n\tbuffer_load_dwordx4 %1, %5, %6 offen lds\n\tbuffer_load_dwordx4 %2, %5, %6 offen offset:1024 lds\n\t"
                 "buffer_load_dwordx4 %3, %5, %6 offen offset:2048 lds\n\tbuffer_load_dwordx4 %4, %5, %6 offen offset:3072 lds"
                 :: "s"(ldsaddr), "v"(v0), "v"(v1), "v"(v2), "v"(v3), "s"(rs), "s"(soff) : "memory");
}
DI void touch(rsrc_t rs, unsigned ldsaddr, unsigned voff) {
    asm volatile("s_mov_b32 m0, %0\n\ts_nop 0\n\tbuffer_load_dword %1, %2, 0 offen lds" :: "s"(ldsaddr), "v"(voff), "s"(rs) : "memory");
}
template <int N> DI void wait_vm() { asm volatile("s_waitcnt vmcnt(%0)" :: "i"(N) : "memory"); }
DI void wait_lgkm0() { asm volatile("s_waitcnt lgkmcnt(0)" ::: "memory"); }
DI void wg_barrier() { asm volatile("s_waitcnt lgkmcnt(0)\n\ts_barrier" ::: "memory"); }
#define SCHED_FENCE() __builtin_amdgcn_sched_barrier(0)
template <int NM, int NR> DI void interleave() {
#pragma unroll
    for (int i = 0; i < (NM > NR ? NM : NR); ++i) {
        if (i < NM) __builtin_amdgcn_sched_group_barrier(0x8, 1, 0);
        if (i < NR) __builtin_amdgcn_sched_group_barrier(0x100, 1, 0);
    }
}
template <int... I> struct iseq {};
template <int N, int... I> struct mkseq : mkseq<N - 1, N - 1, I...> {};
template <int... I> struct mkseq<0, I...> { typedef iseq<I...> type; };

typedef volatile LAS unsigned lflag;
DI unsigned lf_ld(LAS char* lds, int off) { return __builtin_amdgcn_readfirstlane(*(lflag*)(lds + L_FLAG + off)); }
DI void lf_st(LAS char* lds, int off, unsigned v) { *(lflag*)(lds + L_FLAG + off) = v; }
DI void lf_wait(LAS char* lds, int off, unsigned v) {
#pragma unroll 1
    for (unsigned spin = 0; spin < (1u << 22); ++spin) { if (lf_ld(lds, off) >= v) break; __builtin_amdgcn_s_sleep(0); }
}
DI unsigned lf_min4(LAS char* lds, int off) {
    const v4u d = *(volatile LAS v4u*)(lds + L_FLAG + off);
    return __builtin_amdgcn_readfirstlane(min(min(d.x, d.y), min(d.z, d.w)));
}
DI void lf_wait4(LAS char* lds, int off, unsigned v) {
#pragma unroll 1
    for (unsigned spin = 0; spin < (1u << 22); ++spin) { if (lf_min4(lds, off) >= v) break; __builtin_amdgcn_s_sleep(0); }
}

typedef GAS u64 gu64;
DI void publish(char* ws, unsigned off, u64 tag, int lane) {
    if (lane == 0) __hip_atomic_store((gu64*)(ws + off), tag, __ATOMIC_RELAXED, __HIP_MEMORY_SCOPE_AGENT);
}
DI void poll_tags(char* ws, unsigned off, bool act, u64 tag) {
#pragma unroll 1
    for (unsigned spin = 0; spin < (1u << 16); ++spin) {
        u64 v = tag;
        if (act) v = __hip_atomic_load((gu64*)(ws + off), __ATOMIC_RELAXED, __HIP_MEMORY_SCOPE_AGENT);
        if (__all(v == tag)) break;
        __builtin_amdgcn_s_sleep(1);
    }
    asm volatile("" ::: "memory");
}

struct Params {
    const unsigned short *q, *k, *v, *shift, *ssm, *ssmD, *w1, *b1, *w2, *b2, *gw;
    unsigned short* out;
    char* ws;
};

DI f32x2 gelu2(f32x2 u) {
    const float c0 = -2.0f * 0.7978845608028654f * 1.4426950408889634f, c1 = c0 * 0.044715f;
    const f32x2 z = u * (u * u * c1 + c0);
    f32x2 e = {__builtin_amdgcn_exp2f(z.x), __builtin_amdgcn_exp2f(z.y)};
    e = e + 1.0f;
    const f32x2 r = {__builtin_amdgcn_rcpf(e.x), __builtin_amdgcn_rcpf(e.y)};
    return u * r;
}
DI int task_of(int b, int w) { return b < 128 ? 3 * b + w : 384 + 2 * (b - 128) + w; }

DI void ld64_nt(v2u& dst, rsrc_t rs, unsigned voff, int imm) { asm volatile("buffer_load_dwordx2 %0, %1, %2, 0 offen offset:%3 nt" : "=v"(dst) : "v"(voff), "s"(rs), "i"(imm) : "memory"); }
DI void ld128(v4u& dst, rsrc_t rs, unsigned voff, int imm) { asm volatile("buffer_load_dwordx4 %0, %1, %2, 0 offen offset:%3" : "=v"(dst) : "v"(voff), "s"(rs), "i"(imm) : "memory"); }
DI void ld128_nt(v4u& dst, rsrc_t rs, unsigned voff, int imm) { asm volatile("buffer_load_dwordx4 %0, %1, %2, 0 offen offset:%3 nt" : "=v"(dst) : "v"(voff), "s"(rs), "i"(imm) : "memory"); }
DI void ld128_sc1(v4u& dst, rsrc_t rs, unsigned voff, unsigned soff) { asm volatile("buffer_load_dwordx4 %0, %1, %2, %3 offen sc1" : "=v"(dst) : "v"(voff), "s"(rs), "s"(soff) : "memory"); }
#define PIN(x) asm volatile("" : "+v"(x))

struct G1Ld { rsrc_t rx, rw; unsigned xv[4], wv[12], ldsb; LAS char* lds; };
DI void g1_ld_x(const G1Ld& c, int ch) {
    dma4(c.rx, c.ldsb + L_X + ch * X_CH, c.xv[0], c.xv[1], c.xv[2], c.xv[3], ch * 128);
    dma4(c.rx, c.ldsb + L_X + ch * X_CH + 4096, c.xv[0], c.xv[1], c.xv[2], c.xv[3], ch * 128 + 32 * (D_ * 2));
}
DI void g1_ld_w(const G1Ld& c, int u, int slot) {
#pragma unroll
    for (int g = 0; g < 3; ++g) dma4(c.rw, c.ldsb + L_W + slot * W_SL + g * 4096, c.wv[4 * g], c.wv[4 * g + 1], c.wv[4 * g + 2], c.wv[4 * g + 3], u * 32 * (F_ * 2));
}
constexpr int g1_item_n(int i) { return i < 9 ? (i % 3 == 0 ? 8 : 12) : 8; }
constexpr int g1_cum(int i) { int n = 0; for (int e = 0; e <= i; ++e) n += g1_item_n(e); return n; }
constexpr int g1_steps_in(int n) {
    int s = 0; for (int i = 0; i < 9; ++i) if (i % 3 != 0 && g1_cum(i) <= n) ++s; return s; }
template <int I> DI void g1_init_item(const G1Ld& c) {
    if constexpr (I < 9) { if constexpr (I % 3 == 0) g1_ld_x(c, I / 3); else g1_ld_w(c, 2 * (I / 3) + (I % 3) - 1, 2 * (I / 3) + (I % 3) - 1); }
    else g1_ld_x(c, I - 6);
    constexpr int n = g1_cum(I) - 44, np = I > 0 ? g1_cum(I - 1) - 44 : -1;
    if constexpr (n > 0 && g1_steps_in(n) > g1_steps_in(np > 0 ? np : 0)) { wait_vm<44>(); lf_st(c.lds, F_LANDED, g1_steps_in(n)); }
}
template <int... I> DI void g1_init(const G1Ld& c, iseq<I...>) { (g1_init_item<I>(c), ...); }

struct G2Ctx {
    rsrc_t rh; unsigned hoff; LAS char* trp; LAS char* lds; int w; unsigned lv;
    f32x4 acc[5][4]; v4u h[4][4]; v4u wf[2][5];
};
template <int S> DI void g2_issue_h(G2Ctx& c) {
#pragma unroll
    for (int T = 0; T < 4; ++T) ld128_sc1(c.h[S % 4][T], c.rh, c.hoff, T * 49152 + S * 1024);
}
template <int S> DI void g2_read_w(G2Ctx& c) {
#pragma unroll
    for (int t5 = 0; t5 < 5; ++t5) c.wf[S & 1][t5] = tr_read2(c.trp + (S % G2_NS) * G2_SLOT + 32 * t5, 16 * 160);
}
constexpr int g2_after(int s) { return s <= 9 ? 8 : s == 10 ? 4 : 0; }
template <int S> DI void g2_step(G2Ctx& c) {
    asm volatile("s_waitcnt vmcnt(%4) lgkmcnt(0)" : "+v"(c.h[S % 4][0]), "+v"(c.h[S % 4][1]), "+v"(c.h[S % 4][2]), "+v"(c.h[S % 4][3]) : "i"(g2_after(S)) : "memory");
    lf_st(c.lds, F_DONE2 + 4 * c.w, S + 1);
    if constexpr (S + 3 < 12) g2_issue_h<S + 3>(c);
    if constexpr (S + 1 < 12) { if (c.lv < S + 2) { lf_wait(c.lds, F_LANDED2, S + 2); } }
    SCHED_FENCE();
    if constexpr (S + 1 < 12) { g2_read_w<S + 1>(c); c.lv = *(lflag*)(c.lds + L_FLAG + F_LANDED2); }
#pragma unroll
    for (int t5 = 0; t5 < 5; ++t5)
#pragma unroll
        for (int T = 0; T < 4; ++T) c.acc[t5][T] = mfma16(c.wf[S & 1][t5], c.h[S % 4][T], c.acc[t5][T]);
    if constexpr (S + 1 < 12) interleave<20, 11>();
    SCHED_FENCE();
    if constexpr (S + 1 < 12) c.lv = __builtin_amdgcn_readfirstlane(c.lv);
}
template <int... I> DI void g2_steps(G2Ctx& c, iseq<I...>) { (g2_step<I>(c), ...); }

__global__ void __launch_bounds__(512, 2) k_fused(Params p) {
    extern __shared__ __attribute__((aligned(256))) char lds_[];
    LAS char* lds = (LAS char*)lds_;
    const unsigned ldsb = (unsigned)(uintptr_t)lds;
    const int w = __builtin_amdgcn_readfirstlane(threadIdx.x >> 6), lane = threadIdx.x & 63, b = blockIdx.x;
    STAMP(0); CSTAMP(30);
    if (w == 0 && lane < 16) *(lflag*)(lds + L_FLAG + 4 * lane) = 0;
    const int xcd = b & 7, rr = b >> 3;
    const int r8 = rr >> 2, c4 = rr & 3;
    const int R = 8 * (xcd >> 1) + r8, ct = 4 * (xcd & 1) + c4;
    const rsrc_t rws = make_rsrc(p.ws, WS_END);
    const int j = lane & 15, kq = lane >> 4, q = (lane & 15) >> 2, pq = lane & 3;
    wg_barrier();

    if (w < 4) {
        const int m = w >> 1, n = w & 1;
        {
            const rsrc_t rx = make_rsrc(p.q, S_ * D_ * 2), rw1 = make_rsrc(p.w1, D_ * F_ * 2);
            if (w < 3) { const int idx = 64 * w + lane;
                touch(rx, ldsb + L_DUMMY, idx < 160 ? (64 * R + 16 * c4 + (idx & 15)) * (D_ * 2) + (idx >> 4) * 128 : OOB); }
            { const int idx = 64 * w + lane;
                touch(rw1, ldsb + L_DUMMY, idx < 240 ? (8 * (idx / 3) + r8) * (F_ * 2) + 384 * ct + 128 * (idx % 3) : OOB); }
        }
        const u64 tag = *(const GAS u64*)(p.ws + WS_E) * 6364136223846793005ull + 1442695040888963407ull;
        v2u b1v[6];
#pragma unroll
        for (int z = 0; z < 6; ++z) b1v[z] = *(const GAS v2u*)(p.b1 + 192 * ct + 96 * n + 16 * ((z + 3 * m) % 6) + 4 * kq);
        const int fsw = (j >> 1) & 7;
        const int x0 = L_X + (32 * m + j) * 128 + 16 * (kq ^ fsw), x1 = L_X + (32 * m + j) * 128 + 16 * ((4 + kq) ^ fsw);
        const int sw = ((q >> 1) & 1) | ((kq & 1) << 1);
        int wo[6];
#pragma unroll
        for (int z = 0; z < 6; ++z) { const int blk = 6 * n + (z + 3 * m) % 6, aa = blk >> 2, bb = blk & 3;
            wo[z] = L_W + (8 * kq + q) * 384 + 128 * aa + 32 * (bb ^ sw) + 8 * pq; }
        f32x4 acc[6][2], cs[3];
#pragma unroll
        for (int z = 0; z < 6; ++z) { acc[z][0] = acc[z][1] = (f32x4){0.f, 0.f, 0.f, 0.f}; }
#pragma unroll
        for (int z = 0; z < 3; ++z) cs[z] = (f32x4){0.f, 0.f, 0.f, 0.f};
        const v4u ones = {0x3f803f80u, 0x3f803f80u, 0x3f803f80u, 0x3f803f80u};
        v4u xa[2], wa[6], xb[2], wb[6];
        STAMP(1);
        lf_wait(lds, F_LANDED, 1);
        STAMP(2); CSTAMP(15);
        xa[0] = lds_ld128(lds + x0); xa[1] = lds_ld128(lds + x0 + 2048);
#pragma unroll
        for (int z = 0; z < 6; ++z) wa[z] = tr_read2(lds + wo[z], 4 * 384);
        unsigned lv = 0;
        int sl = 0;
        for (int c = 0; c < 10; ++c) {
            wait_lgkm0();
            lf_st(lds, F_DONE + 4 * w, 2 * c + 1);
            if (lv < (unsigned)(2 * c + 2)) lf_wait(lds, F_LANDED, 2 * c + 2);
            sl = sl == NW - 1 ? 0 : sl + 1;
            SCHED_FENCE();
            {
                LAS char* xs = lds + c * X_CH; LAS char* wsl = lds + sl * W_SL;
                xb[0] = lds_ld128(xs + x1); xb[1] = lds_ld128(xs + x1 + 2048);
#pragma unroll
                for (int z = 0; z < 6; ++z) wb[z] = tr_read2(wsl + wo[z], 4 * 384);
                lv = *(lflag*)(lds + L_FLAG + F_LANDED);
#pragma unroll
                for (int z = 0; z < 6; ++z) { acc[z][0] = mfma16(wa[z], xa[0], acc[z][0]); acc[z][1] = mfma16(wa[z], xa[1], acc[z][1]); if (z < 3) cs[z] = mfma16(wa[z], ones, cs[z]); }
                interleave<15, 15>();
            }
            SCHED_FENCE();
            lv = __builtin_amdgcn_readfirstlane(lv);
            wait_lgkm0();
            lf_st(lds, F_DONE + 4 * w, 2 * c + 2);
            if (c < 9) {
                if (lv < (unsigned)(2 * c + 3)) lf_wait(lds, F_LANDED, 2 * c + 3);
                sl = sl == NW - 1 ? 0 : sl + 1;
            }
            SCHED_FENCE();
            {
                LAS char* xs = lds + (c < 9 ? c + 1 : c) * X_CH; LAS char* wsl = lds + sl * W_SL;
                xa[0] = lds_ld128(xs + x0); xa[1] = lds_ld128(xs + x0 + 2048);
#pragma unroll
                for (int z = 0; z < 6; ++z) wa[z] = tr_read2(wsl + wo[z], 4 * 384);
                lv = *(lflag*)(lds + L_FLAG + F_LANDED);
#pragma unroll
                for (int z = 0; z < 6; ++z) { acc[z][0] = mfma16(wb[z], xb[0], acc[z][0]); acc[z][1] = mfma16(wb[z], xb[1], acc[z][1]); if (z < 3) cs[z] = mfma16(wb[z], ones, cs[z]); }
                interleave<15, 15>();
            }
            SCHED_FENCE();
            lv = __builtin_amdgcn_readfirstlane(lv);
        }
        STAMP(3);
        if (j == 0) {
#pragma unroll
            for (int z = 0; z < 3; ++z) *(LAS f32x4*)(lds + L_CS + ((m * 2 + n) * 3 + z) * 64 + 16 * kq) = cs[z];
        }
#pragma unroll
        for (int z = 0; z < 6; ++z) PIN(b1v[z]);
        wg_barrier();
        STAMP(4);
        {
            f32x4 csf[6];
#pragma unroll
            for (int z = 0; z < 3; ++z) { csf[z] = cs[z]; csf[3 + z] = *(LAS f32x4*)(lds + L_CS + (((1 - m) * 2 + n) * 3 + z) * 64 + 16 * kq); }
            v2u o[6][2];
#pragma unroll
            for (int T = 0; T < 2; ++T) {
                const f32x2 st = *(LAS f32x2*)(lds + L_STATS + 8 * (32 * m + 16 * T + j));
#pragma unroll
                for (int z = 0; z < 6; ++z) {
                    const f32x2 b01 = {bf_lo(b1v[z].x), bf_hi(b1v[z].x)}, b23 = {bf_lo(b1v[z].y), bf_hi(b1v[z].y)};
                    const f32x2 a01 = {acc[z][T][0], acc[z][T][1]}, a23 = {acc[z][T][2], acc[z][T][3]};
                    const f32x2 c01 = {csf[z][0], csf[z][1]}, c23 = {csf[z][2], csf[z][3]};
                    o[z][T] = (v2u){pk_bf16v(gelu2(a01 * st.x + (c01 * st.y + b01))), pk_bf16v(gelu2(a23 * st.x + (c23 * st.y + b23)))};
                }
#pragma unroll
                for (int zp = 0; zp < 3; ++zp) {
                    const v2u lo = m ? o[(2 * zp + 3) % 6][T] : o[2 * zp][T], hi = m ? o[(2 * zp + 4) % 6][T] : o[2 * zp + 1][T];
                    __builtin_amdgcn_raw_buffer_store_b128((v4u){lo.x, lo.y, hi.x, hi.y}, rws,
                        WS_H + ((4 * R + 2 * m + T) * 48 + 6 * ct + 3 * n + zp) * 1024 + lane * 16, 0, AUX_SC1);
                }
            }
        }
        STAMP(5);
        wait_vm<0>();
        publish(p.ws, WS_FH + ((R * 8 + ct) * 4 + w) * 8, tag, lane);
        STAMP(6);
        G2Ctx c;
        c.rh = make_rsrc(p.ws + WS_H + ((size_t)(4 * R) * 48 + 12 * w) * 1024, 4 * 49152);
        c.hoff = lane * 16; c.lds = lds; c.w = w; c.lv = 0;
        c.trp = lds + w * G2_PIECE + (4 * kq + q) * 160 + 8 * pq;
#pragma unroll
        for (int t5 = 0; t5 < 5; ++t5)
#pragma unroll
            for (int T = 0; T < 4; ++T) c.acc[t5][T] = (f32x4){0.f, 0.f, 0.f, 0.f};
        poll_tags(p.ws, WS_FH + ((R * 8 + 2 * w) * 4 + (lane & 7)) * 8, lane < 8, tag);
        STAMP(7);
        g2_issue_h<0>(c); g2_issue_h<1>(c); g2_issue_h<2>(c);
        lf_wait(lds, F_LANDED2, 1);
        g2_read_w<0>(c);
        g2_steps(c, mkseq<12>::type{});
        STAMP(8);
#pragma unroll
        for (int T = 0; T < 4; ++T) {
            if (T != w) {
                const int slot = T < w ? T : T - 1;
#pragma unroll
                for (int t5 = 0; t5 < 5; ++t5) *(LAS f32x4*)(lds + slot * G2_SLOT + w * G2_PIECE + t5 * 1024 + lane * 16) = c.acc[t5][T];
            }
        }
        STAMP(9);
        wg_barrier();
        STAMP(10);
        LAS char* fin = w < 3 ? lds + L_CV + w * CV_REG : lds + L_FIN3;
        const float omg = *(LAS float*)(lds + L_OMG + 4 * (16 * w + j));
        const int tok = 64 * R + 16 * w + j;
#pragma unroll
        for (int t5 = 0; t5 < 5; ++t5) {
            f32x4 sacc = w == 0 ? c.acc[t5][0] : w == 1 ? c.acc[t5][1] : w == 2 ? c.acc[t5][2] : c.acc[t5][3];
#pragma unroll
            for (int o = 0; o < 4; ++o)
                if (o != w) { const int slot = w < o ? w : w - 1; sacc += *(LAS f32x4*)(lds + slot * G2_SLOT + o * G2_PIECE + t5 * 1024 + lane * 16); }
            const f32x4 ga = *(LAS f32x4*)(fin + 2560 + t5 * 1024 + lane * 16);
            *(GAS v2u*)(p.out + (size_t)tok * D_ + 80 * ct + 16 * t5 + 4 * kq) =
                (v2u){pk_bf16(fmaf(omg, sacc[0], ga[0]), fmaf(omg, sacc[1], ga[1])), pk_bf16(fmaf(omg, sacc[2], ga[2]), fmaf(omg, sacc[3], ga[3]))};
        }
        STAMP(11); CSTAMP(31);
    } else {
        const int a = w - 4;
        u64 tag;
        float gate_a;
        if (a == 3) {
            __builtin_amdgcn_s_setprio(1);
            G1Ld c;
            c.rx = make_rsrc_biased(p.q, S_ * D_ * 2); c.rw = make_rsrc_biased(p.w1, D_ * F_ * 2); c.ldsb = ldsb; c.lds = lds;
#pragma unroll
            for (int i = 0; i < 4; ++i) { const int row = 8 * i + (lane >> 3), pp = lane & 7; c.xv[i] = (64 * R + row) * (D_ * 2) + 16 * (pp ^ ((row >> 1) & 7)) + BIAS - 1024 * i; }
#pragma unroll
            for (int i = 0; i < 12; ++i) { const int pos = 64 * i + lane, row = pos / 24, cc = pos % 24, blk = cc >> 1, aa = blk >> 2, bb = blk & 3;
                const int sw = ((row >> 1) & 1) | (((row >> 3) & 1) << 1);
                c.wv[i] = row * (F_ * 2) + 384 * ct + 16 * (8 * aa + 2 * (bb ^ sw) + (cc & 1)) + BIAS - 1024 * (i & 3); }
            { const rsrc_t rg = make_rsrc(p.gw, D_ * 2); dma1(rg, ldsb + L_GATE, lane * 16, 0); if (lane < 16) dma1(rg, ldsb + L_GATE + 1024, 1024 + lane * 16, 0); }
            STAMP(1);
            g1_init(c, mkseq<16>::type{});
            STAMP(2);
            tag = *(const GAS u64*)(p.ws + WS_E) * 6364136223846793005ull + 1442695040888963407ull;
            int slot = 0;
            for (int u = NW; u < 20; ++u) {
                lf_wait4(lds, F_DONE, u - NW + 1);
                g1_ld_w(c, u, slot);
                slot = slot == NW - 1 ? 0 : slot + 1;
                if (u >= 9) { wait_vm<36>(); lf_st(lds, F_LANDED, u - 2); }
            }
            wait_vm<24>(); lf_st(lds, F_LANDED, 18);
            wait_vm<12>(); lf_st(lds, F_LANDED, 19);
            wait_vm<0>(); lf_st(lds, F_LANDED, 20);
            STAMP(3);
            wg_barrier();
            STAMP(5);
            {
                const rsrc_t rw2 = make_rsrc_biased(p.w2, F_ * D_ * 2);
                unsigned wv[5];
#pragma unroll
                for (int i = 0; i < 5; ++i) { const int pos = 64 * i + lane, row = pos / 10, cc = pos % 10; wv[i] = row * (D_ * 2) + 160 * ct + 16 * cc + BIAS - (i < 4 ? 1024 * i : 0); }
                auto issue = [&](int u) {
                    const unsigned base = ldsb + (u % G2_NS) * G2_SLOT;
#pragma unroll
                    for (int ww = 0; ww < 4; ++ww) {
                        const unsigned so = (384 * ww + 32 * u) * (D_ * 2);
                        dma4(rw2, base + ww * G2_PIECE, wv[0], wv[1], wv[2], wv[3], so);
                        dma1(rw2, base + ww * G2_PIECE + 4096, wv[4], so);
                    }
                };
                issue(0); issue(1); issue(2);
                wait_vm<40>(); lf_st(lds, F_LANDED2, 1);
                issue(3);
                wait_vm<40>(); lf_st(lds, F_LANDED2, 2);
                wait_vm<20>(); lf_st(lds, F_LANDED2, 3);
                wait_vm<0>(); lf_st(lds, F_LANDED2, 4);
                STAMP(6);
                if (b == 0) {
#pragma unroll
                    for (int i = 0; i < 4; ++i) poll_tags(p.ws, WS_FH + (64 * i + lane) * 32, true, tag);
                    if (lane == 0) __hip_atomic_store((gu64*)(p.ws + WS_E), tag, __ATOMIC_RELAXED, __HIP_MEMORY_SCOPE_AGENT);
                }
                for (int u = G2_NS; u < 12; ++u) {
                    lf_wait4(lds, F_DONE2, u - G2_NS + 1);
                    issue(u);
                    if (u > G2_NS) { wait_vm<20>(); lf_st(lds, F_LANDED2, u); }
                }
                wait_vm<0>(); lf_st(lds, F_LANDED2, 12);
                STAMP(7);
            }
            gate_a = 1.0f - *(LAS float*)(lds + L_OMG + 4 * (48 + j));
        } else {
            const bool has = a < (b < 128 ? 3 : 2);
            const int task = task_of(b, a);
            __builtin_amdgcn_s_setprio(2);
            v2u kk[11], vv[8]; v4u sA, sB;
            const int cht = task % 10, tt = task / 10, cg = lane & 15, tg = lane >> 4;
            const int pch = 64 * cht + 4 * cg, tb = 32 * tt + 8 * tg;
            {
                const rsrc_t rk = make_rsrc(p.k, S_ * D_ * 2), rv = make_rsrc(p.v, S_ * D_ * 2), rs = make_rsrc(p.shift, D_ * 8);
                const unsigned off0 = has ? tb * (D_ * 2) + pch * 2 : OOB;
                const unsigned offh = (has && tb > 0) ? off0 - 3 * (D_ * 2) : OOB;
                const unsigned off1 = off0 + 4 * (D_ * 2), offs = has ? pch * 8 : OOB;
                ld64_nt(kk[0], rk, offh, 0); ld64_nt(kk[1], rk, offh, 1280); ld64_nt(kk[2], rk, offh, 2560);
                ld64_nt(kk[3], rk, off0, 0); ld64_nt(kk[4], rk, off0, 1280); ld64_nt(kk[5], rk, off0, 2560); ld64_nt(kk[6], rk, off0, 3840);
                ld64_nt(kk[7], rk, off1, 0); ld64_nt(kk[8], rk, off1, 1280); ld64_nt(kk[9], rk, off1, 2560); ld64_nt(kk[10], rk, off1, 3840);
                ld64_nt(vv[0], rv, off0, 0); ld64_nt(vv[1], rv, off0, 1280); ld64_nt(vv[2], rv, off0, 2560); ld64_nt(vv[3], rv, off0, 3840);
                ld64_nt(vv[4], rv, off1, 0); ld64_nt(vv[5], rv, off1, 1280); ld64_nt(vv[6], rv, off1, 2560); ld64_nt(vv[7], rv, off1, 3840);
                ld128(sA, rs, offs, 0); ld128(sB, rs, offs, 16);
            }
            __builtin_amdgcn_s_setprio(0);
            STAMP(1);
            tag = *(const GAS u64*)(p.ws + WS_E) * 6364136223846793005ull + 1442695040888963407ull;
            asm volatile("s_waitcnt vmcnt(0)" : "+v"(kk[0]), "+v"(kk[1]), "+v"(kk[2]), "+v"(kk[3]), "+v"(kk[4]), "+v"(kk[5]), "+v"(kk[6]), "+v"(kk[7]), "+v"(kk[8]), "+v"(kk[9]), "+v"(kk[10]),
                         "+v"(vv[0]), "+v"(vv[1]), "+v"(vv[2]), "+v"(vv[3]), "+v"(vv[4]), "+v"(vv[5]), "+v"(vv[6]), "+v"(vv[7]), "+v"(sA), "+v"(sB) :: "memory");
            STAMP(2);
            {
                const float s[4][4] = {{bf_lo(sA.x), bf_hi(sA.x), bf_lo(sA.y), bf_hi(sA.y)}, {bf_lo(sA.z), bf_hi(sA.z), bf_lo(sA.w), bf_hi(sA.w)},
                                       {bf_lo(sB.x), bf_hi(sB.x), bf_lo(sB.y), bf_hi(sB.y)}, {bf_lo(sB.z), bf_hi(sB.z), bf_lo(sB.w), bf_hi(sB.w)}};
                const unsigned ko = has ? WS_KVR + pch * (S_ * 2) + (2040 - tb) * 2 : OOB;
#pragma unroll
                for (int cp = 0; cp < 2; ++cp) {
                    f32x2 kf[11], kv[8];
#pragma unroll
                    for (int i = 0; i < 11; ++i) { const unsigned u = cp ? kk[i].y : kk[i].x; kf[i] = (f32x2){bf_lo(u), bf_hi(u)}; }
                    const f32x2 s0 = {s[2 * cp][0], s[2 * cp + 1][0]}, s1 = {s[2 * cp][1], s[2 * cp + 1][1]}, s2 = {s[2 * cp][2], s[2 * cp + 1][2]}, s3 = {s[2 * cp][3], s[2 * cp + 1][3]};
#pragma unroll
                    for (int t = 0; t < 8; ++t) { const unsigned u = cp ? vv[t].y : vv[t].x;
                        kv[t] = (s0 * kf[t + 3] + s1 * kf[t + 2] + s2 * kf[t + 1] + s3 * kf[t]) * (f32x2){bf_lo(u), bf_hi(u)}; }
                    const v4u o0 = {pk_bf16(kv[7].x, kv[6].x), pk_bf16(kv[5].x, kv[4].x), pk_bf16(kv[3].x, kv[2].x), pk_bf16(kv[1].x, kv[0].x)};
                    const v4u o1 = {pk_bf16(kv[7].y, kv[6].y), pk_bf16(kv[5].y, kv[4].y), pk_bf16(kv[3].y, kv[2].y), pk_bf16(kv[1].y, kv[0].y)};
                    __builtin_amdgcn_raw_buffer_store_b128(o0, rws, ko + (2 * cp) * (S_ * 2), 0, AUX_SC1);
                    __builtin_amdgcn_raw_buffer_store_b128(o1, rws, ko + (2 * cp + 1) * (S_ * 2), 0, AUX_SC1);
                }
            }
            STAMP(3);
            v4u hw[4], hn[4]; v4u h0v;
            {
                const rsrc_t rh = make_rsrc(p.ssm, S_ * D_ * 2);
                const unsigned ho = has ? task * 4096 + 16 * lane : OOB, h0o = has ? task * 4096 : OOB;
                ld128_nt(hw[0], rh, ho, 0); ld128_nt(hn[0], rh, ho, 16); ld128_nt(hw[1], rh, ho, 1024); ld128_nt(hn[1], rh, ho, 1040);
                ld128_nt(hw[2], rh, ho, 2048); ld128_nt(hn[2], rh, ho, 2064); ld128_nt(hw[3], rh, ho, 3072); ld128_nt(hn[3], rh, ho, 3088);
                ld128_nt(h0v, rh, h0o, 0);
            }
            wait_vm<9>();
            if (has) publish(p.ws, WS_FKV + (cht * 64 + tt) * 8, tag, lane);
            STAMP(4);
            if (a < 2) {
                const rsrc_t rw2 = make_rsrc(p.w2, F_ * D_ * 2);
#pragma unroll
                for (int i = 0; i < 2; ++i) { const int idx = 128 * a + 64 * i + lane;
                    touch(rw2, ldsb + L_DUMMY, idx < 240 ? (48 * rr + idx / 5) * (D_ * 2) + 640 * (xcd & 1) + 128 * (idx % 5) : OOB); }
            }
            lf_wait(lds, F_LANDED, 7);
            const int ntile = a == 2 ? 2 : 1;
            for (int tl = 0; tl < ntile; ++tl) {
                const int xrow = 16 * (a + tl) + j, fsw = (xrow >> 1) & 7;
                LAS char* xp0 = lds + L_X + xrow * 128 + 16 * (kq ^ fsw);
                LAS char* xp1 = lds + L_X + xrow * 128 + 16 * ((4 + kq) ^ fsw);
                float s1 = 0.f, s2 = 0.f, sg = 0.f;
#pragma unroll 2
                for (int c = 0; c < 10; ++c) {
                    const v4u xx[2] = {lds_ld128(xp0 + c * X_CH), lds_ld128(xp1 + c * X_CH)};
                    const v4u gg[2] = {lds_ld128(lds + L_GATE + c * 128 + kq * 16), lds_ld128(lds + L_GATE + c * 128 + 64 + kq * 16)};
#pragma unroll
                    for (int s = 0; s < 2; ++s) {
                        const v4u x = xx[s], gw = gg[s];
                        s1 = dot2bf(x.x, 0x3f803f80u, s1); s1 = dot2bf(x.y, 0x3f803f80u, s1); s1 = dot2bf(x.z, 0x3f803f80u, s1); s1 = dot2bf(x.w, 0x3f803f80u, s1);
                        s2 = dot2bf(x.x, x.x, s2); s2 = dot2bf(x.y, x.y, s2); s2 = dot2bf(x.z, x.z, s2); s2 = dot2bf(x.w, x.w, s2);
                        sg = dot2bf(x.x, gw.x, sg); sg = dot2bf(x.y, gw.y, sg); sg = dot2bf(x.z, gw.z, sg); sg = dot2bf(x.w, gw.w, sg);
                    }
                }
                s1 += __shfl_xor(s1, 16); s2 += __shfl_xor(s2, 16); sg += __shfl_xor(sg, 16);
                s1 += __shfl_xor(s1, 32); s2 += __shfl_xor(s2, 32); sg += __shfl_xor(sg, 32);
                const float mean = s1 * (1.0f / D_), var = fmaxf(s2 * (1.0f / D_) - mean * mean, 0.f), rstd = 1.0f / sqrtf(var + 1e-5f);
                const float gate = 1.0f / (1.0f + __expf(-sg));
                if (tl == 0) gate_a = gate;
                if (kq == 0) {
                    *(LAS f32x2*)(lds + L_STATS + 8 * xrow) = (f32x2){rstd, -mean * rstd};
                    *(LAS float*)(lds + L_OMG + 4 * xrow) = 1.0f - gate;
                }
            }
            STAMP(5);
            asm volatile("s_waitcnt vmcnt(0)" : "+v"(hw[0]), "+v"(hw[1]), "+v"(hw[2]), "+v"(hw[3]), "+v"(hn[0]), "+v"(hn[1]), "+v"(hn[2]), "+v"(hn[3]), "+v"(h0v) :: "memory");
            wg_barrier();
            STAMP(6);
            if (has) {
                const int d = task;
                LAS char* reg = lds + L_CV + a * CV_REG;
#pragma unroll
                for (int i = 0; i < 4; ++i) {
                    const unsigned aa[6] = {hw[i].x, hw[i].y, hw[i].z, hw[i].w, hn[i].x, hn[i].y};
                    LAS char* dst = reg + 32 + 1024 * i + 16 * lane;
                    *(LAS v4u*)(dst) = hw[i];
                    *(LAS v4u*)(dst + CV_COPY) = (v4u){__builtin_amdgcn_alignbit(aa[1], aa[0], 16), __builtin_amdgcn_alignbit(aa[2], aa[1], 16), __builtin_amdgcn_alignbit(aa[3], aa[2], 16), __builtin_amdgcn_alignbit(aa[4], aa[3], 16)};
                    *(LAS v4u*)(dst + 2 * CV_COPY) = (v4u){aa[1], aa[2], aa[3], aa[4]};
                    *(LAS v4u*)(dst + 3 * CV_COPY) = (v4u){__builtin_amdgcn_alignbit(aa[2], aa[1], 16), __builtin_amdgcn_alignbit(aa[3], aa[2], 16), __builtin_amdgcn_alignbit(aa[4], aa[3], 16), __builtin_amdgcn_alignbit(aa[5], aa[4], 16)};
                }
                if (lane < 8) {
                    const int rho = lane >> 1;
                    v4u z = {0, 0, 0, 0};
                    if (lane & 1) {
                        if (rho == 1) z.w = h0v.x << 16;
                        if (rho == 2) z.w = h0v.x;
                        if (rho == 3) { z.z = h0v.x << 16; z.w = __builtin_amdgcn_alignbit(h0v.y, h0v.x, 16); }
                    }
                    *(LAS v4u*)(reg + rho * CV_COPY + 16 * (lane & 1)) = z;
                }
                if (lane < 30) *(LAS v4u*)(reg + CV_XR + 4096 + 16 * lane) = (v4u){0, 0, 0, 0};
                const unsigned dv = p.ssmD[d];
                STAMP(7);
                poll_tags(p.ws, WS_FKV + ((d >> 6) * 64 + lane) * 8, true, tag);
                STAMP(8);
                v4u x[4];
#pragma unroll
                for (int i = 0; i < 4; ++i) x[i] = __builtin_amdgcn_raw_buffer_load_b128(rws, WS_KVR + d * 4096 + 1024 * i + 16 * lane, 0, AUX_SC1);
#pragma unroll
                for (int i = 0; i < 4; ++i) *(LAS v4u*)(reg + CV_XR + 1024 * i + 16 * lane) = x[i];
                wait_lgkm0();
                STAMP(9);
                const int pp = 1 + j + 8 * kq, rho = pp & 3;
                LAS char* abase = reg + rho * CV_COPY + 2 * (pp - rho);
                LAS char* bbase = reg + CV_XR + 4064 - 32 * j + 16 * kq - 3584;
                f32x4 acc[8];
                v4u A[2][8], B[2][8];
#pragma unroll
                for (int m = 0; m < 8; ++m) {
                    const v2u lo = lds_ld64v(abase + 512 * m), hi = lds_ld64v(abase + 512 * m + 8);
                    A[0][m] = (v4u){lo.x, lo.y, hi.x, hi.y};
                    B[0][m] = lds_ld128(bbase + 512 * (7 - m));
                }
                {
                    const int jj = 15 - j - 8 * kq;
                    const unsigned val = (jj & 1) ? (dv << 16) : dv;
                    v4u ad;
                    ad.x = (jj >> 1) == 0 ? val : 0; ad.y = (jj >> 1) == 1 ? val : 0; ad.z = (jj >> 1) == 2 ? val : 0; ad.w = (jj >> 1) == 3 ? val : 0;
                    if (jj < 0) ad = (v4u){0, 0, 0, 0};
#pragma unroll
                    for (int c = 0; c < 8; ++c) acc[c] = mfma16(ad, B[0][c], (f32x4){0.f, 0.f, 0.f, 0.f});
                }
                for (int d2 = 0; d2 < 4; ++d2) {
#pragma unroll
                    for (int hf = 0; hf < 2; ++hf) {
                        SCHED_FENCE();
                        LAS char* an = abase + 64 * (2 * d2 + hf + 1);
                        LAS char* bn = bbase + 64 * (2 * d2 + hf + 1);
#pragma unroll
                        for (int m = 0; m < 8; ++m) {
                            const v2u lo = lds_ld64v(an + 512 * m), hi = lds_ld64v(an + 512 * m + 8);
                            A[hf ^ 1][m] = (v4u){lo.x, lo.y, hi.x, hi.y};
                            B[hf ^ 1][m] = lds_ld128(bn + 512 * (7 - m));
                        }
#pragma unroll
                        for (int c = 0; c < 8; ++c)
#pragma unroll
                            for (int m = 0; m <= c; ++m) acc[c] = mfma16(A[hf][m], B[hf][c - m], acc[c]);
                        interleave<36, 24>();
                        SCHED_FENCE();
                    }
                }
                STAMP(10);
#pragma unroll
                for (int c = 0; c < 8; ++c) {
                    const v2u o = {pk_bf16(acc[c][0], acc[c][1]), pk_bf16(acc[c][2], acc[c][3])};
                    __builtin_amdgcn_raw_buffer_store_b64(o, rws, WS_Y + (d * S_ + 256 * c + 16 * j + 4 * kq) * 2, 0, AUX_SC1);
                }
                wait_vm<0>();
                publish(p.ws, WS_FY + d * 8, tag, lane);
                STAMP(11);
            }
        }
        {
            LAS char* fin = a < 3 ? lds + L_CV + a * CV_REG : lds + L_FIN3;
            v2u qv[5], bv[5];
#pragma unroll
            for (int t5 = 0; t5 < 5; ++t5) {
                qv[t5] = *(const GAS v2u*)(p.q + (size_t)(64 * R + 16 * a + j) * D_ + 80 * ct + 16 * t5 + 4 * kq);
                bv[t5] = *(const GAS v2u*)(p.b2 + 80 * ct + 16 * t5 + 4 * kq);
            }
            poll_tags(p.ws, WS_FY + (80 * ct + lane) * 8, true, tag);
            poll_tags(p.ws, WS_FY + (80 * ct + 64 + (lane & 15)) * 8, lane < 16, tag);
            STAMP(12);
            v4u y[3];
#pragma unroll
            for (int i = 0; i < 3; ++i) { const int pc = 64 * i + lane;
                y[i] = __builtin_amdgcn_raw_buffer_load_b128(rws, pc < 160 ? WS_Y + ((80 * ct + (pc >> 1)) * S_ + 64 * R + 16 * a) * 2 + 16 * (pc & 1) : OOB, 0, AUX_SC1); }
#pragma unroll
            for (int i = 0; i < 3; ++i) { const int pc = 64 * i + lane; if (pc < 160) *(LAS v4u*)(fin + 16 * pc) = y[i]; }
            wait_lgkm0();
            const float omg = 1.0f - gate_a;
#pragma unroll
            for (int t5 = 0; t5 < 5; ++t5) {
                const v2u yv = tr_read(fin + (16 * t5 + 4 * kq + q) * 32 + 8 * pq);
                const float qf[4] = {bf_lo(qv[t5].x), bf_hi(qv[t5].x), bf_lo(qv[t5].y), bf_hi(qv[t5].y)}, yf[4] = {bf_lo(yv.x), bf_hi(yv.x), bf_lo(yv.y), bf_hi(yv.y)},
                            bf[4] = {bf_lo(bv[t5].x), bf_hi(bv[t5].x), bf_lo(bv[t5].y), bf_hi(bv[t5].y)};
                f32x4 ga;
#pragma unroll
                for (int r = 0; r < 4; ++r) ga[r] = fmaf(gate_a, qf[r] * yf[r], omg * bf[r]);
                *(LAS f32x4*)(fin + 2560 + t5 * 1024 + lane * 16) = ga;
            }
            STAMP(13);
        }
        wg_barrier();
    }
}

extern "C" void kernel_launch(void* const* d_in, const int* in_sizes, int n_in, void* d_out, int out_size, void* d_ws, size_t ws_size, hipStream_t stream) {
    Params p;
    p.q = (const unsigned short*)d_in[0]; p.k = (const unsigned short*)d_in[1]; p.v = (const unsigned short*)d_in[2];
    p.shift = (const unsigned short*)d_in[3]; p.ssm = (const unsigned short*)d_in[4]; p.ssmD = (const unsigned short*)d_in[5];
    p.w1 = (const unsigned short*)d_in[7]; p.b1 = (const unsigned short*)d_in[8]; p.w2 = (const unsigned short*)d_in[9]; p.b2 = (const unsigned short*)d_in[10];
    p.gw = (const unsigned short*)d_in[11];
    p.out = (unsigned short*)d_out; p.ws = (char*)d_ws;
    (void)hipFuncSetAttribute((const void*)k_fused, hipFuncAttributeMaxDynamicSharedMemorySize, L_END);
    k_fused<<<256, 512, L_END, stream>>>(p);
}
```

```cpp
#include <hip/hip_runtime.h>
#include <stdint.h>

#define GAS __attribute__((address_space(1)))
#define LAS __attribute__((address_space(3)))
#define DI __device__ __forceinline__

typedef unsigned v4u __attribute__((ext_vector_type(4)));
typedef unsigned v2u __attribute__((ext_vector_type(2)));
typedef float f32x4 __attribute__((ext_vector_type(4)));
typedef float f32x2 __attribute__((ext_vector_type(2)));
typedef __bf16 bf16x8 __attribute__((ext_vector_type(8)));
typedef __bf16 bf16x2 __attribute__((ext_vector_type(2)));
typedef short s16x4 __attribute__((ext_vector_type(4)));
typedef unsigned long long u64;
typedef __amdgpu_buffer_rsrc_t rsrc_t;

constexpr int S_ = 2048, D_ = 640, F_ = 1536;
constexpr int AUX_SC1 = 16;
constexpr unsigned OOB = 0x80000000u;
constexpr unsigned Y_NONE = 0xffffffffu;
constexpr unsigned BIAS = 4096;

#define Y_ONEP 1
#define E_Y 1
#define H4 1
#define G2N 1
#define W5S 0
#define HV1 1
#define WPRE 4
#define WL_EARLY 2
#define SKIPV 1
#define H_FIRST 1
#define PRIO_F 2
#define PRIO_L3 2
#define KI_GATE 1
#define B2_EARLY 1
#define CE_EARLY 1
#define TAIL2 3
#define FV_T 2
#define DUMP_FIRST 1
#define PRIO_L1 0
#define DMA_G 1
#define PRIO_GL 1
#define PRIO_G2 1
#define FW_EARLY 0
#define CW_GATE 1
#define RED64 0
#define YS_W3 -1
#define CV_NOP2 7
#define G1_PAT 7
#define FA_EARLY 0
#define KV_T 0
#define KV_B 0
#define DG_GATE 1
#define H_AUX 16
#define KV_AUX 16
#define YS_AUX 16
#define RL_AUX 16
#define POS_K -1
#define POS_V -1
#define POS_KV0 0
#define KV_PER 4
#define POS_H 5
#define POS_PUB 2
#define X_NT 0
#define W_NT 0
#define R_NT 1
#define R_PER 2
#define FH_JOIN 1
#define FW_ST 128
#define FKV_ST 8
#define FY_ST 8
#define W45 1
#define FIN_TRIM 1
#define PRE_H 0
#define Y_TOUCH 0
#define Y_HINT 0
#define POS_ST -1
#define POS_RA -1
#define POS_RB 0
#define POS_RP 3
#define PRIO_G 1
#define PRIO_E 1
#define PRIO_L 2
#define PRIO_C 0
#define PRIO_W 0
#define PRIO_C2 2
#define PRIO_L2 0
#define DLY_W 0
#define DLY_L 0
#define DLY_T 90
#define G2ROT 0
#define HINT_MAX 64
#define HINT_SLP 1
#define HINT_SLPY 8
#define Y_AT 5
#define Y_WAIT 4
#define Y_SLP 8
#define HINT_H 1
#define HINT_Y 1
#define POS_CP 9
#define HST_END 0
#define LD_GATE F_HS
#define CV_GATE 1
#define OUT_AUX 0
#define YS_N 16
#define CODE_PF 30720
#define ROT 31
#define TOUCH 1
#define Y_SENT 0
#define POS_YF 9
#define DUMMY_N 0
#define Y_ALIAS 0
#define FW_SLP 8
#define KV_SLP 8
#define YP_IT 1
#define FY_SLP 12
#define POS_YP 10
#define F_DMA 0
#define JB 1

static_assert(!(Y_ALIAS && Y_SENT), "the sentinels would overwrite kv");
static_assert(POS_K <= POS_KV0 && POS_V <= POS_KV0 && (POS_CP > 8 || (POS_H <= POS_KV0 && POS_CP > POS_KV0 && POS_CP + 2 <= 8)), "worker schedule");
static_assert(!E_Y || (FY_ST == 8 && !Y_SENT), "epoch");
static_assert(!Y_ONEP || (FY_ST == 8 && Y_WAIT == 4), "one poller");
constexpr unsigned WS_E    = 0;
constexpr unsigned WS_FKV  = FKV_ST == 8 ? 4096 : 32768 + 256 * 128 + 640 * 128;
constexpr unsigned WS_FW   = 16384;
constexpr unsigned WS_FH   = 32768;
constexpr unsigned WS_FY   = WS_FH + 256 * 128;
constexpr unsigned WS_KVR  = WS_FY + 640 * 128 + 640 * 128;
constexpr unsigned KV_ROW = KV_B ? 1024 : S_ * 2;
constexpr unsigned WS_H    = WS_KVR + D_ * S_ * 2;
constexpr unsigned WS_Y    = WS_H + S_ * F_ * 2;
constexpr unsigned WS_W2T  = WS_Y + D_ * S_ * 2;
constexpr unsigned WS_END  = WS_W2T + F_ * D_ * 2;

constexpr int CH_SZ = 32768, CH_W = 8192, W_ST = 12288;
constexpr int G2_PIECE = 5120, G2_SLOT = 4 * G2_PIECE;
constexpr int g2_slot(int i) { return i < 3 ? i * G2_SLOT : 131072; }
constexpr int CV_COPY = 4160, CV_XR = 4 * CV_COPY, CV_REG = 21248;
constexpr int L_CV = 65536;
constexpr int Y_ROW = 144, L_YT = 3 * G2_SLOT, L_GQ = L_YT + 80 * Y_ROW, L_W45 = L_GQ + 3 * 5120;
static_assert(L_W45 + 2 * G2_SLOT <= 131072, "lds map");
constexpr int L_STG = 131072;
constexpr int L_FIN3 = 131072 + G2_SLOT;
constexpr int L_GATE = L_FIN3;
constexpr int L_ZERO = L_GATE + 2560;
constexpr int L_B1 = L_GATE + 3072;
constexpr int L_ONES = L_B1 + 1024;
constexpr int L_STATS = L_FIN3 + 7680;
constexpr int L_GT = L_STATS + 512;
constexpr int L_CS = L_GT + 256;
constexpr int L_FLAG = L_CS + 768;
constexpr int L_B2F = L_FLAG + 64;
constexpr int L_END = 163840;
constexpr int F_LANDED2 = 4, F_CG = 8, F_CW = 12, F_G2 = 20, F_W2OK = 24, F_HS = 28, F_YS = 32, F_HJ = 36, F_W45 = 40, F_B2 = 44, F_KI = 16, F_CE = 0;
static_assert(7680 + 80 * Y_ROW <= CV_REG && L_CV + 3 * CV_REG <= 131072 && L_ONES + 256 <= L_STATS && L_B2F + 320 <= L_END && 3 * G2_SLOT <= L_CV, "lds map");

#define STAMP(idx) do {} while (0)
#define CSTAMP(idx) do {} while (0)
#define STFLUSH() do {} while (0)

#define LSTAMP(i) do {} while (0)
#define PIN(x) asm volatile("" : "+v"(x))
DI f32x4 mfma16(v4u a, v4u b, f32x4 c) {
    return __builtin_amdgcn_mfma_f32_16x16x32_bf16(__builtin_bit_cast(bf16x8, a), __builtin_bit_cast(bf16x8, b), c, 0, 0, 0);
}
DI unsigned pk_bf16(float lo, float hi) { f32x2 v = {lo, hi}; return __builtin_bit_cast(unsigned, __builtin_convertvector(v, bf16x2)); }
DI unsigned pk_bf16v(f32x2 v) { return __builtin_bit_cast(unsigned, __builtin_convertvector(v, bf16x2)); }
DI float bf_lo(unsigned u) { return __uint_as_float(u << 16); }
DI float bf_hi(unsigned u) { return __uint_as_float(u & 0xffff0000u); }
DI rsrc_t make_rsrc(const void* p, unsigned bytes) { return __builtin_amdgcn_make_buffer_rsrc((void*)p, 0, bytes, 0x00020000); }
DI rsrc_t make_rsrc_biased(const void* p, unsigned bytes) { return __builtin_amdgcn_make_buffer_rsrc((void*)((const char*)p - BIAS), 0, bytes + BIAS, 0x00020000); }
DI v2u tr_read(LAS char* p) { return __builtin_bit_cast(v2u, __builtin_amdgcn_ds_read_tr16_b64_v4i16((LAS s16x4*)p)); }
DI v4u tr_read2(LAS char* p, int d) { const v2u lo = tr_read(p), hi = tr_read(p + d); return (v4u){lo.x, lo.y, hi.x, hi.y}; }
DI v4u lds_ld128(LAS char* p) { return *(LAS v4u*)p; }
DI v2u lds_ld64v(LAS char* p) { return *(volatile LAS v2u*)p; }
DI void dma1_nt(rsrc_t rs, unsigned ldsaddr, unsigned voff, unsigned soff) {
    asm volatile("s_mov_b32 m0, %0\n\ts_nop 0\n\tbuffer_load_dwordx4 %1, %2, %3 offen nt lds" :: "s"(ldsaddr), "v"(voff), "s"(rs), "s"(soff) : "memory");
}
DI void dma1(rsrc_t rs, unsigned ldsaddr, unsigned voff, unsigned soff) {
    asm volatile("s_mov_b32 m0, %0\n\ts_nop 0\n\tbuffer_load_dwordx4 %1, %2, %3 offen lds" :: "s"(ldsaddr), "v"(voff), "s"(rs), "s"(soff) : "memory");
}
DI void dma4(rsrc_t rs, unsigned ldsaddr, unsigned v0, unsigned v1, unsigned v2, unsigned v3, unsigned soff) {
    asm volatile("s_mov_b32 m0, %0\n\ts_nop 0\n\tbuffer_load_dwordx4 %1, %5, %6 offen lds\n\tbuffer_load_dwordx4 %2, %5, %6 offen offset:1024 lds\n\t"
                 "buffer_load_dwordx4 %3, %5, %6 offen offset:2048 lds\n\tbuffer_load_dwordx4 %4, %5, %6 offen offset:3072 lds"
                 :: "s"(ldsaddr), "v"(v0), "v"(v1), "v"(v2), "v"(v3), "s"(rs), "s"(soff) : "memory");
}
template <int LO, int GO> DI void dma4g(rsrc_t rs, unsigned sb, unsigned s, unsigned v0, unsigned v1, unsigned v2, unsigned v3) {
    unsigned so;
    asm volatile("s_add_u32 m0, %1, %7\n\ts_add_u32 %0, %2, %8\n\tbuffer_load_dwordx4 %3, %9, %0 offen lds\n\tbuffer_load_dwordx4 %4, %9, %0 offen offset:1024 lds\n\t"
                 "buffer_load_dwordx4 %5, %9, %0 offen offset:2048 lds\n\tbuffer_load_dwordx4 %6, %9, %0 offen offset:3072 lds"
                 : "=&s"(so) : "s"(sb), "s"(s), "v"(v0), "v"(v1), "v"(v2), "v"(v3), "i"(LO), "i"(GO), "s"(rs) : "memory", "scc");
}
DI void dma4_sc1(rsrc_t rs, unsigned ldsaddr, unsigned v) {
    asm volatile("s_mov_b32 m0, %0\n\ts_nop 0\n\tbuffer_load_dwordx4 %1, %2, 0 offen sc1 lds\n\tbuffer_load_dwordx4 %1, %2, 0 offen offset:1024 sc1 lds\n\t"
                 "buffer_load_dwordx4 %1, %2, 0 offen offset:2048 sc1 lds\n\tbuffer_load_dwordx4 %1, %2, 0 offen offset:3072 sc1 lds"
                 :: "s"(ldsaddr), "v"(v), "s"(rs) : "memory");
}
DI void dma4_in(rsrc_t rs, unsigned ldsaddr, unsigned v) {
    asm volatile("s_mov_b32 m0, %0\n\ts_nop 0\n\tbuffer_load_dwordx4 %1, %2, 0 offen lds\n\tbuffer_load_dwordx4 %1, %2, 0 offen offset:1024 lds\n\t"
                 "buffer_load_dwordx4 %1, %2, 0 offen offset:2048 lds\n\tbuffer_load_dwordx4 %1, %2, 0 offen offset:3072 lds"
                 :: "s"(ldsaddr), "v"(v), "s"(rs) : "memory");
}
DI void dma5_sc1(rsrc_t rs, unsigned ldsaddr, unsigned v, unsigned soff, unsigned soff4) {
    asm volatile("s_mov_b32 m0, %0\n\ts_nop 0\n\tbuffer_load_dwordx4 %1, %2, %3 offen sc1 lds\n\tbuffer_load_dwordx4 %1, %2, %3 offen offset:1024 sc1 lds\n\t"
                 "buffer_load_dwordx4 %1, %2, %3 offen offset:2048 sc1 lds\n\tbuffer_load_dwordx4 %1, %2, %3 offen offset:3072 sc1 lds\n\t"
                 "s_add_u32 m0, m0, 0x1000\n\ts_nop 0\n\tbuffer_load_dwordx4 %1, %2, %4 offen sc1 lds"
                 :: "s"(ldsaddr), "v"(v), "s"(rs), "s"(soff), "s"(soff4) : "memory", "scc");
}
template <int N> DI void wait_vm() { asm volatile("s_waitcnt vmcnt(%0)" :: "i"(N) : "memory"); }
DI void wait_lgkm0() { asm volatile("" ::: "memory"); __builtin_amdgcn_s_waitcnt(0xC07F); asm volatile("" ::: "memory"); }
DI void wg_barrier() { wait_lgkm0(); asm volatile("s_barrier" ::: "memory"); }
DI void wg_barrier_unless(unsigned skip) {
    wait_lgkm0();
    asm volatile("s_cmp_lg_u32 %0, 0\n\ts_cbranch_scc1 1f\n\ts_barrier\n1:" :: "s"(skip) : "scc", "memory");
}
#define SCHED_FENCE() __builtin_amdgcn_sched_barrier(0)
template <int N, int MASK> DI void sgb() { __builtin_amdgcn_sched_group_barrier(MASK, N, 0); }
template <int NM, int NR> DI void interleave() {
#pragma unroll
    for (int i = 0; i < (NM > NR ? NM : NR); ++i) {
        if (i < NM) __builtin_amdgcn_sched_group_barrier(0x8, 1, 0);
        if (i < NR) __builtin_amdgcn_sched_group_barrier(0x100, 1, 0);
    }
}
template <int... I> struct iseq {};
template <int N, int... I> struct mkseq : mkseq<N - 1, N - 1, I...> {};
template <int... I> struct mkseq<0, I...> { typedef iseq<I...> type; };

typedef volatile LAS unsigned lflag;
DI void lf_st(LAS char* lds, int off, unsigned v) { *(lflag*)(lds + L_FLAG + off) = v; }
template <int OFF> DI void lf_wait(unsigned fa, unsigned v, unsigned have = 0) {
    unsigned t, cnt;
    asm volatile("s_cmp_ge_u32 %2, %3\n\ts_cbranch_scc1 2f\n\ts_mov_b32 %1, 0x400000\n"
                 "1:\n\tds_read_b32 %0, %4 offset:%5\n\ts_waitcnt lgkmcnt(0)\n\tv_readfirstlane_b32 %2, %0\n\ts_cmp_ge_u32 %2, %3\n\ts_cbranch_scc1 2f\n\t"
                 "s_sub_u32 %1, %1, 1\n\ts_cmp_lg_u32 %1, 0\n\ts_cbranch_scc1 1b\n2:"
                 : "=&v"(t), "=&s"(cnt), "+&s"(have) : "s"(v), "v"(fa), "i"(OFF) : "scc", "memory");
}
template <int OFF> DI void lf_wait_h(unsigned fa, unsigned v, unsigned& have) {
    unsigned t, cnt;
    asm volatile("s_cmp_ge_u32 %2, %3\n\ts_cbranch_scc1 2f\n\ts_mov_b32 %1, 0x400000\n"
                 "1:\n\tds_read_b32 %0, %4 offset:%5\n\ts_waitcnt lgkmcnt(0)\n\tv_readfirstlane_b32 %2, %0\n\ts_cmp_ge_u32 %2, %3\n\ts_cbranch_scc1 2f\n\t"
                 "s_sub_u32 %1, %1, 1\n\ts_cmp_lg_u32 %1, 0\n\ts_cbranch_scc1 1b\n2:"
                 : "=&v"(t), "=&s"(cnt), "+&s"(have) : "s"(v), "v"(fa), "i"(OFF) : "scc", "memory");
}
template <int OFF> DI void lf_wait_n(unsigned fa, unsigned v, unsigned n) {
    unsigned t, cnt, have;
    asm volatile("s_mov_b32 %1, %6\n"
                 "1:\n\tds_read_b32 %0, %4 offset:%5\n\ts_waitcnt lgkmcnt(0)\n\tv_readfirstlane_b32 %2, %0\n\ts_cmp_ge_u32 %2, %3\n\ts_cbranch_scc1 2f\n\t"
                 "s_sub_u32 %1, %1, 1\n\ts_cmp_lg_u32 %1, 0\n\ts_cbranch_scc1 1b\n2:"
                 : "=&v"(t), "=&s"(cnt), "=&s"(have) : "s"(v), "v"(fa), "i"(OFF), "s"(n) : "scc", "memory");
}
template <int OFF> DI void lf_wait_slow(unsigned fa, unsigned v) {
    unsigned t, cnt, have;
    asm volatile("s_mov_b32 %1, 0x40000\n"
                 "1:\n\tds_read_b32 %0, %4 offset:%5\n\ts_waitcnt lgkmcnt(0)\n\tv_readfirstlane_b32 %2, %0\n\ts_cmp_ge_u32 %2, %3\n\ts_cbranch_scc1 2f\n\t"
                 "s_sleep 2\n\ts_sub_u32 %1, %1, 1\n\ts_cmp_lg_u32 %1, 0\n\ts_cbranch_scc1 1b\n2:"
                 : "=&v"(t), "=&s"(cnt), "=&s"(have) : "s"(v), "v"(fa), "i"(OFF) : "scc", "memory");
}
template <int OFF> DI void lf_arrive(unsigned fa) {
    u64 sv; const unsigned one = 1;
    wait_lgkm0();
    asm volatile("s_mov_b64 %0, exec\n\ts_mov_b64 exec, 1\n\tds_add_u32 %1, %2 offset:%3\n\ts_mov_b64 exec, %0" : "=&s"(sv) : "v"(fa), "v"(one), "i"(OFF) : "memory");
}
DI void publish(rsrc_t rws, unsigned off, u64 tag) {
    u64 sv; v2u d = {(unsigned)tag, (unsigned)(tag >> 32)};
    asm volatile("s_mov_b64 %0, exec\n\ts_mov_b64 exec, 1\n\tbuffer_store_dwordx2 %1, %2, %3, 0 offen sc1\n\ts_mov_b64 exec, %0"
                 : "=&s"(sv) : "v"(d), "v"(off), "s"(rws) : "memory");
}
template <int OFF, int N> DI void publish_last(unsigned fa, rsrc_t rws, unsigned off, u64 tag) {
    u64 sv; unsigned t, old; const unsigned one = 1; v2u d = {(unsigned)tag, (unsigned)(tag >> 32)};
    asm volatile("s_mov_b64 %0, exec\n\ts_mov_b64 exec, 1\n\tds_add_rtn_u32 %1, %3, %4 offset:%8\n\ts_waitcnt lgkmcnt(0)\n\tv_readfirstlane_b32 %2, %1\n\t"
                 "s_cmp_lg_u32 %2, %9\n\ts_cbranch_scc1 1f\n\tbuffer_store_dwordx2 %5, %6, %7, 0 offen sc1\n1:\ts_mov_b64 exec, %0"
                 : "=&s"(sv), "=&v"(t), "=&s"(old) : "v"(fa), "v"(one), "v"(d), "v"(off), "s"(rws), "i"(OFF), "i"(N - 1) : "scc", "memory");
}
DI void poll_tags(rsrc_t rws, unsigned off, u64 tag) {
    v2u t; unsigned cnt;
    asm volatile("s_mov_b32 %1, 0x10000\n"
                 "1:\n\tbuffer_load_dwordx2 %0, %2, %3, 0 offen sc1\n\ts_waitcnt vmcnt(0)\n\tv_cmp_ne_u64 vcc, %4, %0\n\ts_cbranch_vccz 2f\n\t"
                 "s_sleep 1\n\ts_sub_u32 %1, %1, 1\n\ts_cmp_lg_u32 %1, 0\n\ts_cbranch_scc1 1b\n2:"
                 : "=&v"(t), "=&s"(cnt) : "v"(off), "s"(rws), "s"(tag) : "scc", "vcc", "memory");
}
typedef u64 v8q __attribute__((ext_vector_type(8)));
typedef u64 v4q __attribute__((ext_vector_type(4)));
DI void hint8(const char* flags, u64 tag) {
#pragma unroll 1
    for (int n = 0; n < HINT_MAX; ++n) {
        v8q f;
        asm volatile("s_load_dwordx16 %0, %1, 0x0 glc\n\ts_waitcnt lgkmcnt(0)" : "=&s"(f) : "s"(flags) : "memory");
        const u64 d = ((f[0] ^ tag) | (f[1] ^ tag)) | ((f[2] ^ tag) | (f[3] ^ tag)) | ((f[4] ^ tag) | (f[5] ^ tag)) | ((f[6] ^ tag) | (f[7] ^ tag));
        if (d == 0) break;
        __builtin_amdgcn_s_sleep(HINT_SLP);
    }
}
DI bool hint2(const char* flags, u64 tag) {
    bool ok = false;
#pragma unroll 1
    for (int n = 0; n < HINT_MAX; ++n) {
        u64 f0, f1;
        asm volatile("s_load_dwordx2 %0, %2, 0x0 glc\n\ts_load_dwordx2 %1, %2, 0x80 glc\n\ts_waitcnt lgkmcnt(0)" : "=&s"(f0), "=&s"(f1) : "s"(flags) : "memory");
        if (((f0 ^ tag) | (f1 ^ tag)) == 0) { ok = true; break; }
        __builtin_amdgcn_s_sleep(HINT_SLP);
    }
    return ok;
}
DI bool hint20(const char* flags, u64 tag) {
    bool ok = false;
#pragma unroll 1
    for (int n = 0; n < HINT_MAX; ++n) {
        v4q f0, f1, f2, f3, f4;
        asm volatile("s_load_dwordx8 %0, %5, 0x0 glc\n\ts_load_dwordx8 %1, %5, 0x20 glc\n\ts_load_dwordx8 %2, %5, 0x40 glc\n\t"
                     "s_load_dwordx8 %3, %5, 0x60 glc\n\ts_load_dwordx8 %4, %5, 0x80 glc\n\ts_waitcnt lgkmcnt(0)"
                     : "=&s"(f0), "=&s"(f1), "=&s"(f2), "=&s"(f3), "=&s"(f4) : "s"(flags) : "memory");
        u64 d = 0;
#pragma unroll
        for (int i = 0; i < 4; ++i) d |= (f0[i] ^ tag) | (f1[i] ^ tag) | (f2[i] ^ tag) | (f3[i] ^ tag) | (f4[i] ^ tag);
        if (d == 0) { ok = true; break; }
        __builtin_amdgcn_s_sleep(HINT_SLPY);
    }
    return ok;
}
template <int SLP> DI void poll_tags_slow(rsrc_t rws, unsigned off, u64 tag) {
    v2u t; unsigned cnt;
    asm volatile("s_mov_b32 %1, 0x4000\n"
                 "1:\n\tbuffer_load_dwordx2 %0, %2, %3, 0 offen sc1\n\ts_waitcnt vmcnt(0)\n\tv_cmp_ne_u64 vcc, %4, %0\n\ts_cbranch_vccz 2f\n\t"
                 "s_sleep %5\n\ts_sub_u32 %1, %1, 1\n\ts_cmp_lg_u32 %1, 0\n\ts_cbranch_scc1 1b\n2:"
                 : "=&v"(t), "=&s"(cnt) : "v"(off), "s"(rws), "s"(tag), "i"(SLP) : "scc", "vcc", "memory");
}
template <int SLP> DI void poll_tags_slow_unless(rsrc_t rws, unsigned off, u64 tag, unsigned skip) {
    v2u t; unsigned cnt;
    asm volatile("s_cmp_lg_u32 %6, 0\n\ts_cbranch_scc1 2f\n\ts_mov_b32 %1, 0x4000\n"
                 "1:\n\tbuffer_load_dwordx2 %0, %2, %3, 0 offen sc1\n\ts_waitcnt vmcnt(0)\n\tv_cmp_ne_u64 vcc, %4, %0\n\ts_cbranch_vccz 2f\n\t"
                 "s_sleep %5\n\ts_sub_u32 %1, %1, 1\n\ts_cmp_lg_u32 %1, 0\n\ts_cbranch_scc1 1b\n2:"
                 : "=&v"(t), "=&s"(cnt) : "v"(off), "s"(rws), "s"(tag), "i"(SLP), "s"(skip) : "scc", "vcc", "memory");
}
template <int OFF> DI void lf_signal(unsigned fa) {
    u64 sv; const unsigned one = 1;
    asm volatile("s_mov_b64 %0, exec\n\ts_mov_b64 exec, 1\n\tds_add_u32 %1, %2 offset:%3\n\ts_mov_b64 exec, %0" : "=&s"(sv) : "v"(fa), "v"(one), "i"(OFF) : "memory");
}
template <int OFF> DI void lf_signal_if(unsigned fa, unsigned a, unsigned b) {
    u64 sv; const unsigned one = 1;
    asm volatile("s_cmp_lg_u32 %4, %5\n\ts_cbranch_scc1 1f\n\ts_mov_b64 %0, exec\n\ts_mov_b64 exec, 1\n\tds_add_u32 %1, %2 offset:%3\n\ts_mov_b64 exec, %0\n1:"
                 : "=&s"(sv) : "v"(fa), "v"(one), "i"(OFF), "s"(a), "s"(b) : "scc", "memory");
}
DI void poll_tags2(rsrc_t rws, unsigned off0, unsigned off1, u64 tag) {
    v2u t, u; unsigned cnt;
    asm volatile("s_mov_b32 %2, 0x10000\n"
                 "1:\n\tbuffer_load_dwordx2 %0, %3, %5, 0 offen sc1\n\tbuffer_load_dwordx2 %1, %4, %5, 0 offen sc1\n\ts_waitcnt vmcnt(0)\n\t"
                 "v_cmp_ne_u64 vcc, %6, %0\n\ts_cbranch_vccnz 3f\n\tv_cmp_ne_u64 vcc, %6, %1\n\ts_cbranch_vccz 2f\n"
                 "3:\n\ts_sleep 1\n\ts_sub_u32 %2, %2, 1\n\ts_cmp_lg_u32 %2, 0\n\ts_cbranch_scc1 1b\n2:"
                 : "=&v"(t), "=&v"(u), "=&s"(cnt) : "v"(off0), "v"(off1), "s"(rws), "s"(tag) : "scc", "vcc", "memory");
}
DI void look_tags(v2u& t, rsrc_t rws, unsigned off) { asm volatile("buffer_load_dwordx2 %0, %1, %2, 0 offen sc1" : "=v"(t) : "v"(off), "s"(rws) : "memory"); }
DI u64 load_tag(const char* ws) {
    u64 e;
    asm volatile("s_load_dwordx2 %0, %1, 0x0\n\ts_waitcnt lgkmcnt(0)" : "=&s"(e) : "s"(ws) : "memory");
    return e * 6364136223846793005ull + 1442695040888963407ull;
}

DI u64 load_epoch(const char* ws) { u64 e; asm volatile("s_load_dwordx2 %0, %1, 0x0" : "=&s"(e) : "s"(ws) : "memory"); return e; }
DI u64 tag_ready(u64 e0) {
    u64 e;
    asm volatile("s_waitcnt lgkmcnt(0)\n\ts_mov_b64 %0, %1" : "=s"(e) : "s"(e0) : "memory");
    return e * 6364136223846793005ull + 1442695040888963407ull;
}
struct Params {
    const unsigned short *q, *k, *v, *shift, *ssm, *ssmD, *w1, *b1, *w2, *b2, *gw;
    unsigned short* out;
    char* ws;
};

typedef unsigned v16u __attribute__((ext_vector_type(16)));
typedef unsigned v8u __attribute__((ext_vector_type(8)));
struct KTouch { unsigned a, b, c; };
static_assert(sizeof(Params) == 104, "kernel argument layout");
DI void ka_touch(KTouch& t, unsigned& b) {
    asm volatile("s_load_dword %0, %4, 0x0\n\ts_load_dword %1, %4, 0x34\n\ts_load_dword %2, %4, 0x64"
                 : "=&s"(t.a), "=&s"(t.b), "=&s"(t.c), "+s"(b) : "s"(__builtin_amdgcn_kernarg_segment_ptr()));
}
DI u64 ka_ptr(unsigned lo, unsigned hi) { return ((u64)hi << 32) | lo; }
DI Params ka_load(const KTouch& t) {
    struct { v16u a; v8u b; v2u c; } k;
    asm volatile("s_load_dwordx16 %0, %3, 0x0\n\ts_load_dwordx8 %1, %3, 0x40\n\ts_load_dwordx2 %2, %3, 0x60\n\ts_waitcnt lgkmcnt(0)"
                 : "=&s"(k.a), "=&s"(k.b), "=&s"(k.c) : "s"(__builtin_amdgcn_kernarg_segment_ptr()), "s"(t.a), "s"(t.b), "s"(t.c) : "memory");
    typedef const unsigned short* cp;
    Params p;
    p.q = (cp)ka_ptr(k.a[0], k.a[1]); p.k = (cp)ka_ptr(k.a[2], k.a[3]); p.v = (cp)ka_ptr(k.a[4], k.a[5]); p.shift = (cp)ka_ptr(k.a[6], k.a[7]);
    p.ssm = (cp)ka_ptr(k.a[8], k.a[9]); p.ssmD = (cp)ka_ptr(k.a[10], k.a[11]); p.w1 = (cp)ka_ptr(k.a[12], k.a[13]); p.b1 = (cp)ka_ptr(k.a[14], k.a[15]);
    p.w2 = (cp)ka_ptr(k.b[0], k.b[1]); p.b2 = (cp)ka_ptr(k.b[2], k.b[3]); p.gw = (cp)ka_ptr(k.b[4], k.b[5]);
    p.out = (unsigned short*)ka_ptr(k.b[6], k.b[7]); p.ws = (char*)ka_ptr(k.c.x, k.c.y);
    return p;
}

DI f32x2 gelu2(f32x2 u) {
    const float c0 = -2.0f * 0.7978845608028654f * 1.4426950408889634f, c1 = c0 * 0.044715f;
    const f32x2 z = u * (u * u * c1 + c0);
    f32x2 e = {__builtin_amdgcn_exp2f(z.x), __builtin_amdgcn_exp2f(z.y)};
    e = e + 1.0f;
    const f32x2 r = {__builtin_amdgcn_rcpf(e.x), __builtin_amdgcn_rcpf(e.y)};
    return u * r;
}
DI void pin4(v4u& x) { asm volatile("" : "+v"(x)); }
DI void memtime(unsigned long long& t) { asm volatile("s_memtime %0" : "=s"(t)); }
template <int V> struct ic { static constexpr int v = V; };
template <class F> DI void for_intervals(F& f) {
    f(ic<-1>{}); f(ic<0>{}); f(ic<1>{}); f(ic<2>{}); f(ic<3>{}); f(ic<4>{}); f(ic<5>{}); f(ic<6>{}); f(ic<7>{}); f(ic<8>{});
}
DI int task_of(int b, int w) { return b < 128 ? 3 * b + w : 384 + 2 * (b - 128) + w; }

DI void ld64_nt(v2u& dst, rsrc_t rs, unsigned voff, int imm) { asm volatile("buffer_load_dwordx2 %0, %1, %2, 0 offen offset:%3 nt" : "=v"(dst) : "v"(voff), "s"(rs), "i"(imm) : "memory"); }
DI void ld128(v4u& dst, rsrc_t rs, unsigned voff, int imm) { asm volatile("buffer_load_dwordx4 %0, %1, %2, 0 offen offset:%3" : "=v"(dst) : "v"(voff), "s"(rs), "i"(imm) : "memory"); }
DI void ld128_nt(v4u& dst, rsrc_t rs, unsigned voff, int imm) { asm volatile("buffer_load_dwordx4 %0, %1, %2, 0 offen offset:%3 nt" : "=v"(dst) : "v"(voff), "s"(rs), "i"(imm) : "memory"); }
template <int IMM> DI void ld128_sc1(v4u& dst, rsrc_t rs, unsigned voff, unsigned soff) {
    asm volatile("buffer_load_dwordx4 %0, %1, %2, %3 offen offset:%4 sc1" : "=v"(dst) : "v"(voff), "s"(rs), "s"(soff), "i"(IMM) : "memory");
}

DI float dot2bf(unsigned a, unsigned b, float c) { return __builtin_amdgcn_fdot2_f32_bf16(__builtin_bit_cast(bf16x2, a), __builtin_bit_cast(bf16x2, b), c, false); }
DI float sumsq(v4u x, float c) { return dot2bf(x.w, x.w, dot2bf(x.z, x.z, dot2bf(x.y, x.y, dot2bf(x.x, x.x, c)))); }

DI float sum_rows(float v) {
    const auto r = __builtin_amdgcn_permlane32_swap(__float_as_uint(v), __float_as_uint(v), false, false);
    v = __uint_as_float(r[0]) + __uint_as_float(r[1]);
    const auto t = __builtin_amdgcn_permlane16_swap(__float_as_uint(v), __float_as_uint(v), false, false);
    return __uint_as_float(t[0]) + __uint_as_float(t[1]);
}

struct G1Ld { rsrc_t rx, rw; unsigned xv[4], wv[3][4], sx, sw; };
#define X_POL ""
#define W_POL ""
#define G1_L4(a, b, c, d, r, so, pol) "buffer_load_dwordx4 %[" a "], %[" r "], %[" so "] offen" pol " lds\n\tbuffer_load_dwordx4 %[" b "], %[" r "], %[" so "] offen offset:1024" pol " lds\n\t" \
                                 "buffer_load_dwordx4 %[" c "], %[" r "], %[" so "] offen offset:2048" pol " lds\n\tbuffer_load_dwordx4 %[" d "], %[" r "], %[" so "] offen offset:3072" pol " lds\n\t"
#define G1_BAR(n) ".if %c[bar] == " #n "\n\ts_waitcnt vmcnt(%c[cnt])\n\ts_barrier\n\t.endif\n\t"
#define G1_THR
template <int KK, bool BAR> DI void g1_chunk(G1Ld& c, unsigned ldsb) {
    unsigned sx1, sw1, dw;
    asm volatile(
        "s_add_u32 m0, %[lb], %c[sb]\n\ts_add_u32 %[sx1], %[sx], 0xa000\n\t" G1_L4("x0", "x1", "x2", "x3", "rx", "sx", X_POL) G1_BAR(1)
        "s_add_u32 m0, %[lb], %c[sb] + 0x1000\n\ts_add_u32 %[sw1], %[sw], 0x18000\n\t" G1_L4("x0", "x1", "x2", "x3", "rx", "sx1", X_POL) G1_BAR(2)
        "s_add_u32 m0, %[lb], %c[sb] + 0x2000\n\ts_add_u32 %[sx], %[sx], 128\n\t" G1_L4("a0", "a1", "a2", "a3", "rw", "sw", W_POL) G1_BAR(3)
        "s_add_u32 m0, %[lb], %c[sb] + 0x3000\n\ts_cmpk_eq_u32 %[sx], 1280\n\ts_cselect_b32 %[dw], %[wrap], %[step]\n\ts_cselect_b32 %[sx], 0, %[sx]\n\t"
        G1_L4("b0", "b1", "b2", "b3", "rw", "sw", W_POL) G1_BAR(4)
        "s_add_u32 m0, %[lb], %c[sb] + 0x4000\n\ts_nop 0\n\t" G1_L4("c0", "c1", "c2", "c3", "rw", "sw", W_POL) G1_BAR(5)
        G1_THR "s_add_u32 m0, %[lb], %c[sb] + 0x5000\n\ts_add_u32 %[sw], %[sw], %[dw]\n\t" G1_L4("a0", "a1", "a2", "a3", "rw", "sw1", W_POL) G1_BAR(6)
        "s_add_u32 m0, %[lb], %c[sb] + 0x6000\n\ts_nop 0\n\t" G1_L4("b0", "b1", "b2", "b3", "rw", "sw1", W_POL) G1_BAR(7)
        "s_add_u32 m0, %[lb], %c[sb] + 0x7000\n\ts_nop 0\n\t" G1_L4("c0", "c1", "c2", "c3", "rw", "sw1", W_POL)
        : [sx] "+&s"(c.sx), [sw] "+&s"(c.sw), [sx1] "=&s"(sx1), [sw1] "=&s"(sw1), [dw] "=&s"(dw)
        : [x0] "v"(c.xv[0]), [x1] "v"(c.xv[1]), [x2] "v"(c.xv[2]), [x3] "v"(c.xv[3]),
          [a0] "v"(c.wv[0][0]), [a1] "v"(c.wv[0][1]), [a2] "v"(c.wv[0][2]), [a3] "v"(c.wv[0][3]),
          [b0] "v"(c.wv[1][0]), [b1] "v"(c.wv[1][1]), [b2] "v"(c.wv[1][2]), [b3] "v"(c.wv[1][3]),
          [c0] "v"(c.wv[2][0]), [c1] "v"(c.wv[2][1]), [c2] "v"(c.wv[2][2]), [c3] "v"(c.wv[2][3]),
          [rx] "s"(c.rx), [rw] "s"(c.rw), [lb] "s"(ldsb), [step] "s"(64 * (F_ * 2)), [wrap] "s"(-576 * (F_ * 2)),
          [sb] "i"((KK & 3) * CH_SZ), [bar] "i"(BAR ? JB : 0), [cnt] "i"(32 + 4 * JB)
        : "memory", "scc");
}

DI LAS char* la(unsigned a) { return (LAS char*)(uintptr_t)a; }
struct G1Ctx {
    unsigned xo[2][2], wo[6], ga, ginc;
    f32x4 acc[6][2], st, gr, cs[3]; v4u x[2][2], wf[2][6], av[2], ones;
};
template <int Q> DI void g1_reads(G1Ctx& c) {
    constexpr int st = Q & 1, xo = (Q >> 1) * CH_SZ, wo = (Q >> 1) * CH_SZ + CH_W + (Q & 1) * W_ST;
    c.x[st][0] = lds_ld128(la(c.xo[st][0]) + xo);
    c.wf[st][0] = tr_read2(la(c.wo[0]) + wo, 512);
    c.x[st][1] = lds_ld128(la(c.xo[st][1]) + xo);
#pragma unroll
    for (int z = 1; z < 6; ++z) c.wf[st][z] = tr_read2(la(c.wo[z]) + wo, 512);
    c.av[st] = lds_ld128(la(c.ga) + 64 * Q);
}
template <int Q> DI void g1_block(G1Ctx& c, unsigned last) {
    constexpr int st = Q & 1;
    if constexpr (Q == 3) c.ga += c.ginc;
    g1_reads<(Q + 1) & 3>(c);
    if constexpr (Q == 2) {
        c.xo[0][0] ^= 0x10000; c.xo[0][1] ^= 0x10000; c.xo[1][0] ^= 0x10000; c.xo[1][1] ^= 0x10000;
#pragma unroll
        for (int z = 0; z < 6; ++z) c.wo[z] ^= 0x10000;
    }
#pragma unroll
    for (int z = 0; z < 6; ++z) {
        c.acc[z][0] = mfma16(c.wf[st][z], c.x[st][0], c.acc[z][0]);
        if (z < 5 || (Q & 1)) c.acc[z][1] = mfma16(c.wf[st][z], c.x[st][1], c.acc[z][1]);
    }
    c.st = mfma16(c.av[st], c.x[st][0], c.st);
    c.gr = mfma16(c.x[st][0], c.x[st][0], c.gr);
#pragma unroll
    for (int z = 0; z < 3; ++z) c.cs[z] = mfma16(c.wf[st][z], c.ones, c.cs[z]);
    if constexpr (G1_PAT >= 5) {
        constexpr int n2 = (Q & 1) ? 0 : G1_PAT == 5 ? 3 : G1_PAT == 6 ? 5 : 2;
#pragma unroll
        for (int i = 0; i < n2; ++i) { sgb<2, 0x100>(); sgb<1, 0x8>(); }
#pragma unroll
        for (int i = 0; i < 15 - 2 * n2; ++i) { sgb<1, 0x100>(); sgb<1, 0x8>(); if (Q == 2 && i >= 5 - n2) sgb<1, 0x2>(); }
#pragma unroll
        for (int i = 0; i < 2 + n2; ++i) { sgb<1, 0x8>(); if (Q == 2) sgb<1, 0x2>(); }
    } else if constexpr (G1_PAT == 4) {
#pragma unroll
        for (int i = 0; i < 15; ++i) { sgb<1, 0x100>(); sgb<1, 0x8>(); if (Q == 2 && i >= 5) sgb<1, 0x2>(); }
        sgb<2, 0x8>();
    } else if constexpr ((Q & 1) || G1_PAT == 2) {
#pragma unroll
        for (int i = 0; i < 15; ++i) { sgb<1, 0x100>(); sgb<1, 0x8>(); }
        if constexpr (Q == 2) sgb<10, 0x2>();
        sgb<2, 0x8>();
    } else if constexpr (G1_PAT == 3) {
#pragma unroll
        for (int i = 0; i < 5; ++i) { sgb<2, 0x100>(); sgb<1, 0x8>(); sgb<1, 0x100>(); sgb<1, 0x8>(); }
        if constexpr (Q == 2) {
#pragma unroll
            for (int i = 0; i < 5; ++i) { sgb<1, 0x8>(); sgb<2, 0x2>(); }
            sgb<1, 0x8>();
        } else sgb<6, 0x8>();
    } else {
#pragma unroll
        for (int i = 0; i < 7; ++i) { sgb<2, 0x100>(); sgb<1, 0x8>(); }
        sgb<1, 0x100>();
        if constexpr (Q == 2) {
#pragma unroll
            for (int i = 0; i < 5; ++i) { sgb<1, 0x8>(); sgb<2, 0x2>(); }
            sgb<4, 0x8>();
        } else sgb<10, 0x8>();
    }
    SCHED_FENCE();
    if constexpr ((Q & 1) == 0) {
        if constexpr (Q == 0) wg_barrier(); else wg_barrier_unless(last);
        SCHED_FENCE();
        c.acc[5][1] = mfma16(c.wf[st][5], c.x[st][1], c.acc[5][1]);
        SCHED_FENCE();
    }
}

struct CvCtx { f32x4 acc[8]; v4u A[2][8], B[2][8]; };
template <int H> DI void cv_read(CvCtx& c, LAS char* an, LAS char* bn, int m) {
    const v2u lo = lds_ld64v(an + 512 * m), hi = lds_ld64v(an + 512 * m + 8);
    c.A[H][m] = (v4u){lo.x, lo.y, hi.x, hi.y};
    c.B[H][m] = *(volatile LAS v4u*)(bn + 512 * (7 - m));
}
constexpr int tri_c(int i) { int c = 0; while ((c + 1) * (c + 2) / 2 <= i) ++c; return c; }
template <int H, int I> DI void cv_step(CvCtx& c, LAS char* an, LAS char* bn) {
    constexpr int cc = tri_c(I), m = I - cc * (cc + 1) / 2;
    asm volatile("v_mfma_f32_16x16x32_bf16 %0, %1, %2, %0" : "+v"(c.acc[cc]) : "v"(c.A[H][m]), "v"(c.B[H][cc - m]));
    if constexpr (I < 24) {
        constexpr int r = I / 3;
        if constexpr (I % 3 == 0) { const v2u lo = lds_ld64v(an + 512 * r); c.A[H ^ 1][r].x = lo.x; c.A[H ^ 1][r].y = lo.y; }
        else if constexpr (I % 3 == 1) { const v2u hi = lds_ld64v(an + 512 * r + 8); c.A[H ^ 1][r].z = hi.x; c.A[H ^ 1][r].w = hi.y; }
        else c.B[H ^ 1][r] = *(volatile LAS v4u*)(bn + 512 * (7 - r));
    }
}
template <int H, int... I> DI void cv_half(CvCtx& c, LAS char* an, LAS char* bn, iseq<I...>) { (cv_step<H, I>(c, an, bn), ...); }

constexpr int NS = (G2N && W5S) ? 5 : 4;
constexpr int WL0 = NS;
struct G2Ctx {
    rsrc_t rh, rw; unsigned hv[4], wv, fa; LAS char* lw; int rot;
    unsigned ldsb, tok0, col0; int w;
    unsigned a_src[3], a_fin, a_y, oo0, oo4; float omg;
    f32x4 acc[5][4]; v4u h[4][4]; v4u wf[NS][5]; unsigned fv, have45, ht[4];
};
DI int g2_ks(const G2Ctx& c, int s) { const int k = s + c.rot; return k >= 12 ? k - 12 : k; }
template <int S> DI void g2_issue_h(G2Ctx& c) {
    const unsigned so = g2_ks(c, S) * 1024;
#pragma unroll
    for (int T = 0; T < 4; ++T) ld128_sc1<0>(c.h[S % 4][T], c.rh, c.hv[T], so);
}
template <int S> DI void g2_issue_w(G2Ctx& c) {
    const unsigned so = g2_ks(c, S) * G2_PIECE;
    ld128_sc1<0>(c.wf[S % NS][0], c.rw, c.wv, so); ld128_sc1<1024>(c.wf[S % NS][1], c.rw, c.wv, so);
    ld128_sc1<2048>(c.wf[S % NS][2], c.rw, c.wv, so); ld128_sc1<3072>(c.wf[S % NS][3], c.rw, c.wv, so);
    ld128_sc1<0>(c.wf[S % NS][4], c.rw, c.wv, so + 4096);
}
constexpr int W_FIRST = W45 ? 6 : 4;
constexpr int g2_cnt(int t) { return (t + 3 < 12 ? 4 : 0) + (t >= W_FIRST - 3 && t <= 4 ? 5 : 0); }
constexpr int g2_after(int s) {
    int total = 12; for (int t = 0; t < s; ++t) total += g2_cnt(t);
    int hpos = s < 3 ? 4 * (s + 1) : 0;
    if (s >= 3) { hpos = 12; for (int t = 0; t < s - 3; ++t) hpos += g2_cnt(t); hpos += 4; }
    int wpos = 0;
    if (s >= W_FIRST && s <= 7) { wpos = 12; for (int t = 0; t <= s - 3; ++t) wpos += g2_cnt(t); }
    return total - (hpos > wpos ? hpos : wpos);
}
constexpr int g2_after4(int s) {
    int n = 16, hl = s < 4 ? 4 * (s + 1) : 0, wl = 0;
    for (int t = 0; t < s; ++t) {
        if (t >= W_FIRST - 3 && t <= 4) { n += 5; if (t + 3 == s) wl = n; }
        if (t + 4 < 12) { n += 4; if (t + 4 == s) hl = n; }
    }
    return n - (hl > wl ? hl : wl);
}
constexpr int g2_wait(int s) { return H4 ? g2_after4(s) : g2_after(s); }
DI LAS char* fin_base(LAS char* lds, int i);
DI void g2_final_addr(G2Ctx& c) {
    unsigned ln;
    asm volatile("v_mbcnt_lo_u32_b32 %0, -1, 0\n\tv_mbcnt_hi_u32_b32 %0, -1, %0" : "=v"(ln));
    const unsigned j2 = ln & 15, kq2 = ln >> 4;
    c.a_fin = (unsigned)(uintptr_t)fin_base(la(c.ldsb), c.w) + 2560 + ln * 16;
#pragma unroll
    for (int T = 1; T < 4; ++T) c.a_src[T - 1] = c.ldsb + (T - 1) * G2_SLOT + ((c.w - T) & 3) * G2_PIECE + ln * 16;
    c.a_y = c.ldsb + L_YT + (4 * kq2 + (j2 >> 2)) * Y_ROW + 32 * c.w + 8 * (ln & 3);
    const unsigned oo = (c.tok0 + j2) * (D_ * 2) + c.col0;
    c.oo0 = oo + 32 * (kq2 & 1) + 16 * (kq2 >> 1); c.oo4 = oo + 128 + 8 * kq2;
    c.omg = *(LAS float*)la(c.ldsb + L_GT + 4 * (16 * c.w + j2));
}
template <int S> DI void g2_step(G2Ctx& c) {
    constexpr int r = S % 4;
    if constexpr (S < 4 || (S >= W_FIRST && S < 8))
        asm volatile("s_waitcnt vmcnt(%9)" : "+v"(c.h[r][0]), "+v"(c.h[r][1]), "+v"(c.h[r][2]), "+v"(c.h[r][3]),
                     "+v"(c.wf[r][0]), "+v"(c.wf[r][1]), "+v"(c.wf[r][2]), "+v"(c.wf[r][3]), "+v"(c.wf[r][4]) : "i"(g2_wait(S)) : "memory");
    else
        asm volatile("s_waitcnt vmcnt(%4)" : "+v"(c.h[r][0]), "+v"(c.h[r][1]), "+v"(c.h[r][2]), "+v"(c.h[r][3]) : "i"(g2_wait(S)) : "memory");
    if constexpr (!H4 && S + 3 < 12) g2_issue_h<S + 3>(c);
    if constexpr (S >= W_FIRST - 3 && S <= 4) g2_issue_w<S + 3>(c);
    if constexpr (W45 && S == 3) lf_wait<F_W45>(c.fa, 1);
    if constexpr (S == 5) lf_wait<F_LANDED2>(c.fa, 1);
    if constexpr (S == Y_AT) lf_signal<F_G2>(c.fa);
    SCHED_FENCE();
    if constexpr (H4 && S < 8) {
        const unsigned so = g2_ks(c, S + 4) * 1024;
        auto grp = [&](auto TT) {
            constexpr int T = decltype(TT)::v, r0 = T == 0 ? 0 : T + 1, r1 = T + 2;
            if constexpr (S >= 5) {
#pragma unroll
                for (int t5 = r0; t5 < r1; ++t5) c.wf[(S + 3) % 4][t5] = lds_ld128(c.lw + g2_slot(S - 5) + t5 * 1024);
            }
            if constexpr (W45 && (S == 3 || S == 4)) {
#pragma unroll
                for (int t5 = r0; t5 < r1; ++t5) c.wf[(S + 1) % 4][t5] = lds_ld128(c.lw + L_W45 + (S - 3) * G2_SLOT + t5 * 1024);
            }
#pragma unroll
            for (int t5 = 0; t5 < 5; ++t5) c.acc[t5][T] = mfma16(c.wf[r][t5], c.h[r][T], c.acc[t5][T]);
            if constexpr (S >= 5 || (W45 && (S == 3 || S == 4))) { sgb<1, 0x8>(); sgb<1, 0x100>(); sgb<1, 0x8>(); sgb<1, 0x100>(); sgb<3, 0x8>(); }
            SCHED_FENCE();
            ld128_sc1<0>(c.h[r][T], c.rh, c.hv[T], so);
            SCHED_FENCE();
        };
        grp(ic<0>{}); grp(ic<1>{}); grp(ic<2>{}); grp(ic<3>{});
        return;
    }
    if constexpr (S >= 5 && S <= 8) {
#pragma unroll
        for (int t5 = 0; t5 < 5; ++t5) c.wf[(S + 3) % 4][t5] = lds_ld128(c.lw + g2_slot(S - 5) + t5 * 1024);
    }
    if constexpr (S == 3 || S == 4) {
#pragma unroll
        for (int t5 = 0; t5 < 5; ++t5) c.wf[(S + 1) % 4][t5] = lds_ld128(c.lw + L_W45 + (S - 3) * G2_SLOT + t5 * 1024);
    }
    if constexpr (S == 11 && FIN_TRIM) {
#pragma unroll
        for (int T1 = 1; T1 <= 4; ++T1)
#pragma unroll
            for (int t5 = 0; t5 < 5; ++t5) c.acc[t5][T1 & 3] = mfma16(c.wf[r][t5], c.h[r][T1 & 3], c.acc[t5][T1 & 3]);
#pragma unroll
        for (int T = 1; T < 4; ++T)
#pragma unroll
            for (int t5 = 0; t5 < 5; ++t5) *(LAS f32x4*)(c.lw + (T - 1) * G2_SLOT + t5 * 1024) = c.acc[t5][T];
        sgb<10, 0x8>();
#pragma unroll
        for (int i = 0; i < 10; ++i) { sgb<1, 0x8>(); sgb<1, 0x200>(); }
        sgb<5, 0x200>();
    } else {
        if constexpr (S == 10 && FIN_TRIM) g2_final_addr(c);
#pragma unroll
        for (int t5 = 0; t5 < 5; ++t5)
#pragma unroll
            for (int T = 0; T < 4; ++T) c.acc[t5][T] = mfma16(c.wf[r][t5], c.h[r][T], c.acc[t5][T]);
        if constexpr ((S >= 5 && S <= 8) || (W45 && (S == 3 || S == 4))) interleave<20, 5>();
        if constexpr (S == 10 && FIN_TRIM) {
#pragma unroll
            for (int i = 0; i < 20; ++i) { sgb<1, 0x8>(); sgb<2, 0x106>(); }
            PIN(c.a_fin); PIN(c.a_src[0]); PIN(c.a_src[1]); PIN(c.a_src[2]); PIN(c.a_y); PIN(c.oo0); PIN(c.oo4);
        }
    }
    SCHED_FENCE();
}
constexpr int g2n_wp(int t, int T) {
    if (NS == 5) return t == 3 && T == 0 ? 56 : t == 3 && T == 2 ? 57 : t == 4 && T == 0 ? 58 : t == 4 && T == 2 ? 59 : t == 5 && T == 0 ? 60 : -1;
    const int i = 4 * (t - 3) + T;
    return i < 0 || i >= 10 ? -1 : i < 5 ? 48 + i : 56 + i - 5;
}
constexpr int g2n_wait(int s) {
    int n = 16, need = s < 4 ? 4 * (s + 1) : 0;
    for (int t = 0; t < s; ++t)
        for (int T = 0; T < 4; ++T) {
            if (t + 4 < 12) { ++n; if (t + 4 == s) need = n; }
            const int wp = g2n_wp(t, T);
            if (wp >= 0) { ++n; if (wp / 8 == s) need = n; }
        }
    return n - need;
}
constexpr int g2n_lds(int s) {
    if (NS == 5 && TAIL2 == 3) return s == 4 ? 5 : s == 5 ? 6 : s == 6 ? 8 : s == 7 ? 9 : -1;
    if (NS == 5) return s == 4 ? 5 : s == 5 ? 6 : s >= 6 && s <= 9 ? s + 2 : -1;
    return s == 3 ? 4 : s == 4 ? 5 : s >= 5 && s <= (TAIL2 == 4 ? 7 : 8) ? s + 3 : -1;
}
constexpr int g2n_lds2(int s) { return NS == 5 && TAIL2 == 3 ? (s == 6 ? 10 : s == 7 ? 11 : -1) : -1; }
constexpr int TAIL_S = TAIL2 >= 2 ? 12 - TAIL2 : TAIL2 == 1 ? 10 : 12;
constexpr int g2n_need(int s) { const int k = g2n_lds(s); return k == WL0 ? 1 : k == WL0 + 1 ? 2 : k == 8 && !DMA_G ? 3 : 0; }
template <int S> DI void g2n_step(G2Ctx& c) {
    constexpr int r = S % 4, ws = S % NS, lk = g2n_lds(S);
    constexpr bool streamed = S >= WL0 + 2 && S < 8;
    if constexpr (S > TAIL_S) return;
    if constexpr (S == TAIL_S && TAIL_S == 10)
        asm volatile("s_waitcnt vmcnt(0)" : "+v"(c.h[2][0]), "+v"(c.h[2][1]), "+v"(c.h[2][2]), "+v"(c.h[2][3]), "+v"(c.h[3][0]), "+v"(c.h[3][1]), "+v"(c.h[3][2]), "+v"(c.h[3][3]) :: "memory");
    else if constexpr (S == TAIL_S) {}
    else if constexpr (S < NS || streamed)
        asm volatile("s_waitcnt vmcnt(%9)" : "+v"(c.h[r][0]), "+v"(c.h[r][1]), "+v"(c.h[r][2]), "+v"(c.h[r][3]),
                     "+v"(c.wf[ws][0]), "+v"(c.wf[ws][1]), "+v"(c.wf[ws][2]), "+v"(c.wf[ws][3]), "+v"(c.wf[ws][4]) : "i"(g2n_wait(S)) : "memory");
    else
        asm volatile("s_waitcnt vmcnt(%4)" : "+v"(c.h[r][0]), "+v"(c.h[r][1]), "+v"(c.h[r][2]), "+v"(c.h[r][3]) : "i"(g2n_wait(S)) : "memory");
    if constexpr (g2n_need(S) > 0) {
        c.have45 = __builtin_amdgcn_readfirstlane(c.fv);
        lf_wait_h<F_W45>(c.fa, g2n_need(S), c.have45);
    }
    if constexpr (S == Y_AT) lf_signal<F_G2>(c.fa);
    SCHED_FENCE();
    if constexpr (S == TAIL_S && TAIL_S == 8) {
        static_assert(TAIL_S != 8 || NS == 4, "register sets");
        auto tile = [&](auto TT) {
            constexpr int T = decltype(TT)::v, TP = T == 1 ? -1 : T == 0 ? 3 : T - 1;
            if constexpr (T == 1) {
                asm volatile("s_waitcnt vmcnt(4)" : "+v"(c.h[0][0]), "+v"(c.h[0][1]), "+v"(c.h[0][2]), "+v"(c.h[0][3]), "+v"(c.h[1][0]), "+v"(c.h[1][1]), "+v"(c.h[1][2]), "+v"(c.h[1][3]),
                             "+v"(c.h[2][0]), "+v"(c.h[2][1]), "+v"(c.h[2][2]), "+v"(c.h[2][3]) :: "memory");
                SCHED_FENCE();
#pragma unroll
                for (int t5 = 0; t5 < 5; ++t5) c.wf[3][t5] = lds_ld128(c.lw + g2_slot(3) + t5 * 1024);
                if constexpr (!FA_EARLY) g2_final_addr(c);
            }
            if constexpr (TP > 0) {
#pragma unroll
                for (int t5 = 0; t5 < 5; ++t5) *(LAS f32x4*)(c.lw + (TP - 1) * G2_SLOT + t5 * 1024) = c.acc[t5][TP];
            }
#pragma unroll
            for (int k = 0; k < 3; ++k)
#pragma unroll
                for (int t5 = 0; t5 < 5; ++t5) c.acc[t5][T] = mfma16(c.wf[k][t5], c.h[k][T], c.acc[t5][T]);
            if constexpr (T == 1 && FA_EARLY) {
#pragma unroll
                for (int i = 0; i < 5; ++i) { sgb<1, 0x8>(); sgb<1, 0x2>(); sgb<1, 0x100>(); }
                sgb<10, 0x8>();
            } else if constexpr (T == 1) {
#pragma unroll
                for (int i = 0; i < 5; ++i) { sgb<1, 0x8>(); sgb<2, 0x6>(); sgb<1, 0x100>(); }
#pragma unroll
                for (int i = 0; i < 10; ++i) { sgb<1, 0x8>(); sgb<2, 0x2>(); sgb<2, 0x4>(); sgb<1, 0x100>(); }
                PIN(c.a_fin); PIN(c.a_src[0]); PIN(c.a_src[1]); PIN(c.a_src[2]); PIN(c.a_y); PIN(c.oo0); PIN(c.oo4);
            } else {
                sgb<2, 0x8>();
#pragma unroll
                for (int i = 0; i < 4; ++i) { sgb<1, 0x200>(); sgb<3, 0x8>(); }
                sgb<1, 0x200>(); sgb<1, 0x8>();
            }
            SCHED_FENCE();
            asm volatile("s_waitcnt vmcnt(%1)" : "+v"(c.h[3][T]) : "i"(T == 0 ? 0 : 4 - T) : "memory");
            SCHED_FENCE();
#pragma unroll
            for (int t5 = 0; t5 < 5; ++t5) c.acc[t5][T] = mfma16(c.wf[3][t5], c.h[3][T], c.acc[t5][T]);
            SCHED_FENCE();
        };
        tile(ic<1>{}); tile(ic<2>{}); tile(ic<3>{}); tile(ic<0>{});
    } else if constexpr (S == TAIL_S && TAIL_S == 9) {
        constexpr int wa = 9 % NS, wb = 10 % NS, wc = 11 % NS;
        auto tile = [&](auto TT) {
            constexpr int T = decltype(TT)::v, TP = T == 1 ? -1 : T == 0 ? 3 : T - 1;
            asm volatile("s_waitcnt vmcnt(%3)" : "+v"(c.h[1][T]), "+v"(c.h[2][T]), "+v"(c.h[3][T]) : "i"(T == 0 ? 0 : 4 - T) : "memory");
            if constexpr (T == 1 && !FA_EARLY) g2_final_addr(c);
            if constexpr (TP > 0) {
#pragma unroll
                for (int t5 = 0; t5 < 5; ++t5) {
                    if constexpr (RED64) {
                        *(volatile LAS f32x2*)(c.lw + (TP - 1) * G2_SLOT + t5 * 1024) = (f32x2){c.acc[t5][TP][0], c.acc[t5][TP][1]};
                        *(volatile LAS f32x2*)(c.lw + (TP - 1) * G2_SLOT + t5 * 1024 + 8) = (f32x2){c.acc[t5][TP][2], c.acc[t5][TP][3]};
                    } else *(LAS f32x4*)(c.lw + (TP - 1) * G2_SLOT + t5 * 1024) = c.acc[t5][TP];
                }
            }
#pragma unroll
            for (int t5 = 0; t5 < 5; ++t5) c.acc[t5][T] = mfma16(c.wf[wa][t5], c.h[1][T], c.acc[t5][T]);
#pragma unroll
            for (int t5 = 0; t5 < 5; ++t5) c.acc[t5][T] = mfma16(c.wf[wb][t5], c.h[2][T], c.acc[t5][T]);
#pragma unroll
            for (int t5 = 0; t5 < 5; ++t5) c.acc[t5][T] = mfma16(c.wf[wc][t5], c.h[3][T], c.acc[t5][T]);
            if constexpr (T == 1 && FA_EARLY) {
                sgb<15, 0x8>();
            } else if constexpr (T == 1) {
#pragma unroll
                for (int i = 0; i < 15; ++i) { sgb<1, 0x8>(); sgb<2, 0x2>(); sgb<2, 0x4>(); sgb<1, 0x100>(); }
                PIN(c.a_fin); PIN(c.a_src[0]); PIN(c.a_src[1]); PIN(c.a_src[2]); PIN(c.a_y); PIN(c.oo0); PIN(c.oo4);
            } else {
                sgb<2, 0x8>();
                if constexpr (RED64) {
#pragma unroll
                    for (int i = 0; i < 10; ++i) { sgb<1, 0x200>(); sgb<1, 0x8>(); }
                } else {
#pragma unroll
                    for (int i = 0; i < 5; ++i) { sgb<1, 0x200>(); sgb<2, 0x8>(); }
                }
                sgb<3, 0x8>();
            }
            SCHED_FENCE();
        };
        tile(ic<1>{}); tile(ic<2>{}); tile(ic<3>{}); tile(ic<0>{});
    } else if constexpr (S == TAIL_S) {
        constexpr int wa = 10 % NS, wb = 11 % NS;
        auto tile = [&](auto TT) {
            constexpr int T = decltype(TT)::v, TP = T == 1 ? -1 : T == 0 ? 3 : T - 1;
            if constexpr (T == 1) g2_final_addr(c);
            if constexpr (TP > 0) {
#pragma unroll
                for (int t5 = 0; t5 < 5; ++t5) *(LAS f32x4*)(c.lw + (TP - 1) * G2_SLOT + t5 * 1024) = c.acc[t5][TP];
            }
#pragma unroll
            for (int t5 = 0; t5 < 5; ++t5) c.acc[t5][T] = mfma16(c.wf[wa][t5], c.h[2][T], c.acc[t5][T]);
#pragma unroll
            for (int t5 = 0; t5 < 5; ++t5) c.acc[t5][T] = mfma16(c.wf[wb][t5], c.h[3][T], c.acc[t5][T]);
            if constexpr (T == 1) {
#pragma unroll
                for (int i = 0; i < 10; ++i) { sgb<1, 0x8>(); sgb<3, 0x106>(); }
                PIN(c.a_fin); PIN(c.a_src[0]); PIN(c.a_src[1]); PIN(c.a_src[2]); PIN(c.a_y); PIN(c.oo0); PIN(c.oo4);
            } else {
                sgb<2, 0x8>();
#pragma unroll
                for (int i = 0; i < 5; ++i) { sgb<1, 0x200>(); sgb<1, 0x8>(); }
                sgb<3, 0x8>();
            }
            SCHED_FENCE();
        };
        tile(ic<1>{}); tile(ic<2>{}); tile(ic<3>{}); tile(ic<0>{});
    } else if constexpr (S == 11) {
#pragma unroll
        for (int T1 = 1; T1 <= 4; ++T1)
#pragma unroll
            for (int t5 = 0; t5 < 5; ++t5) c.acc[t5][T1 & 3] = mfma16(c.wf[ws][t5], c.h[r][T1 & 3], c.acc[t5][T1 & 3]);
#pragma unroll
        for (int T = 1; T < 4; ++T)
#pragma unroll
            for (int t5 = 0; t5 < 5; ++t5) *(LAS f32x4*)(c.lw + (T - 1) * G2_SLOT + t5 * 1024) = c.acc[t5][T];
        sgb<10, 0x8>();
#pragma unroll
        for (int i = 0; i < 10; ++i) { sgb<1, 0x8>(); sgb<1, 0x200>(); }
        sgb<5, 0x200>();
        SCHED_FENCE();
    } else if constexpr (S == 10) {
        g2_final_addr(c);
#pragma unroll
        for (int t5 = 0; t5 < 5; ++t5)
#pragma unroll
            for (int T = 0; T < 4; ++T) c.acc[t5][T] = mfma16(c.wf[ws][t5], c.h[r][T], c.acc[t5][T]);
#pragma unroll
        for (int i = 0; i < 20; ++i) { sgb<1, 0x8>(); sgb<2, 0x106>(); }
        PIN(c.a_fin); PIN(c.a_src[0]); PIN(c.a_src[1]); PIN(c.a_src[2]); PIN(c.a_y); PIN(c.oo0); PIN(c.oo4);
        SCHED_FENCE();
    } else {
        auto grp = [&](auto TT) {
            constexpr int G = decltype(TT)::v, T = (G + 1) & 3, r0 = G == 0 ? 0 : G + 1, r1 = G + 2;
            constexpr int lk2 = g2n_lds2(S);
            if constexpr (g2n_need(S + 1) > 0 && G == FV_T) c.fv = *(volatile LAS unsigned*)(la(c.fa) + F_W45);
            if constexpr (lk >= 0) {
                constexpr int off = lk < 8 ? L_W45 + (lk - WL0) * G2_SLOT : g2_slot(lk - 8);
#pragma unroll
                for (int t5 = r0; t5 < r1; ++t5) c.wf[lk % NS][t5] = lds_ld128(c.lw + off + t5 * 1024);
            }
            if constexpr (lk2 >= 0) {
#pragma unroll
                for (int t5 = 4 - (r1 - 1); t5 <= 4 - r0; ++t5) c.wf[lk2 % NS][t5] = lds_ld128(c.lw + g2_slot(lk2 - 8) + t5 * 1024);
            }
#pragma unroll
            for (int t5 = 0; t5 < 5; ++t5) c.acc[t5][T] = mfma16(c.wf[ws][t5], c.h[r][T], c.acc[t5][T]);
            if constexpr (lk2 >= 0) { sgb<1, 0x8>(); sgb<1, 0x100>(); sgb<1, 0x8>(); sgb<1, 0x100>(); sgb<1, 0x8>(); sgb<1, 0x100>(); sgb<1, 0x8>(); sgb<1, 0x100>(); sgb<1, 0x8>(); }
            else if constexpr (lk >= 0) { sgb<1, 0x8>(); sgb<1, 0x100>(); sgb<1, 0x8>(); sgb<1, 0x100>(); sgb<3, 0x8>(); }
            SCHED_FENCE();
            if constexpr (S + 4 < 12) {
                if constexpr (HV1) ld128_sc1<0>(c.h[r][T], c.rh, c.wv, c.ht[T] + (S + 4) * 1024);
                else ld128_sc1<0>(c.h[r][T], c.rh, c.hv[T], g2_ks(c, S + 4) * 1024);
            }
            constexpr int wp = g2n_wp(S, G);
            if constexpr (wp >= 0) {
                constexpr int k = wp / 8, pc = wp % 8;
                ld128_sc1<(pc & 3) * 1024>(c.wf[k % NS][pc], c.rw, c.wv, k * G2_PIECE + (pc >> 2) * 4096);
            }
            SCHED_FENCE();
        };
        grp(ic<0>{}); grp(ic<1>{}); grp(ic<2>{}); grp(ic<3>{});
    }
}
template <int... I> DI void g2n_steps(G2Ctx& c, iseq<I...>) { (g2n_step<I>(c), ...); }
template <int... I> DI void g2_steps(G2Ctx& c, iseq<I...>) { (g2_step<I>(c), ...); }

struct FinQ { v2u q[5], b[5]; };
template <bool WITH_B> DI void final_loads(FinQ& f, const Params& p, int R, int ct, int tile, int lane) {
    const int kq = lane >> 4;
    const rsrc_t rq = make_rsrc(p.q, S_ * D_ * 2), rb = make_rsrc(p.b2, D_ * 2);
    const unsigned qo = (64 * R + 16 * tile + (lane & 15)) * (D_ * 2) + 160 * ct + 8 * kq, bo = 160 * ct + 8 * kq;
#pragma unroll
    for (int t5 = 0; t5 < 5; ++t5) {
        asm volatile("buffer_load_dwordx2 %0, %1, %2, 0 offen offset:%3" : "=v"(f.q[t5]) : "v"(qo), "s"(rq), "i"(32 * t5) : "memory");
        if constexpr (WITH_B) asm volatile("buffer_load_dwordx2 %0, %1, %2, 0 offen offset:%3" : "=v"(f.b[t5]) : "v"(bo), "s"(rb), "i"(32 * t5) : "memory");
    }
}
template <bool WITH_B> DI void final_landed(FinQ& f) {
    asm volatile("" : "+v"(f.q[0]), "+v"(f.q[1]), "+v"(f.q[2]), "+v"(f.q[3]), "+v"(f.q[4]) :: "memory");
    if constexpr (WITH_B) asm volatile("" : "+v"(f.b[0]), "+v"(f.b[1]), "+v"(f.b[2]), "+v"(f.b[3]), "+v"(f.b[4]) :: "memory");
}
DI LAS char* fin_base(LAS char* lds, int i) { return lds + (i < 3 ? L_GQ - 2560 + i * 5120 : L_FIN3); }
typedef v4u ElT;
DI void final_prep(const Params& p, rsrc_t rws, unsigned fa, LAS char* lds, const FinQ& f, u64 tag, int R, int ct, int tile, int lane, ElT* el
    ) {
    LAS char* fin = fin_base(lds, tile);
    lf_wait_slow<F_CG>(fa, 4);
    lf_wait_slow<F_YS>(fa, 3);
    const float gate = *(LAS float*)(lds + L_GT + 4 * (16 * tile + (lane & 15)));
#pragma unroll
    for (int t5 = 0; t5 < 5; ++t5)
        *(LAS f32x4*)(fin + 2560 + t5 * 1024 + lane * 16) = (f32x4){bf_lo(f.q[t5].x), bf_hi(f.q[t5].x), bf_lo(f.q[t5].y), bf_hi(f.q[t5].y)} * gate;
    if (!FIN_TRIM && tile == 3 && (lane & 15) == 0) {
#pragma unroll
        for (int t5 = 0; t5 < 5; ++t5) *(LAS f32x4*)(lds + L_B2F + t5 * 64 + (lane >> 4) * 16) = (f32x4){bf_lo(f.b[t5].x), bf_hi(f.b[t5].x), bf_lo(f.b[t5].y), bf_hi(f.b[t5].y)};
    }
    STAMP(8);
    lf_wait_slow<F_G2>(fa, Y_WAIT);
    STAMP(7);
    if (tile == Y_ONEP - 1) {
        const unsigned off = WS_FY + 80 * ct * 8 + 16 * (lane < 40 ? lane : 0), tlo = (unsigned)tag, thi = (unsigned)(tag >> 32);
#pragma unroll 1
        for (int n = 0; n < 0x4000; ++n) {
            v4u t;
            asm volatile("buffer_load_dwordx4 %0, %1, %2, 0 offen sc1\n\ts_waitcnt vmcnt(0)" : "=&v"(t) : "v"(off), "s"(rws) : "memory");
            if (__builtin_amdgcn_ballot_w64((((t.x ^ tlo) | (t.y ^ thi)) | ((t.z ^ tlo) | (t.w ^ thi))) != 0) == 0) break;
            __builtin_amdgcn_s_sleep(Y_SLP);
        }
        lf_signal<F_G2>(fa);
    } else lf_wait_slow<F_G2>(fa, 5);
    if (false)
    poll_tags_slow<Y_SLP>(rws, WS_FY + (80 * ct + 20 * tile + (lane < 20 ? lane : 0)) * FY_ST, tag);
    STAMP(12);
    lf_wait<F_YS>(fa, 3);
    v4u y[3];
#pragma unroll
    for (int i = 0; i < 3; ++i) { const int pc = 64 * i + lane;
        ld128_sc1<0>(y[i], rws, pc < 160 ? WS_Y + ((80 * ct + 20 * tile + (pc >> 3)) * S_ + 64 * R) * 2 + 16 * (pc & 7) : OOB, 0); }
    if (el) {
#pragma unroll
        for (int i = 0; i < 5; ++i) ld128_sc1<0>(el[i], rws, WS_FY + 1024 * i + 16 * lane, 0);
        asm volatile("s_waitcnt vmcnt(5)" : "+v"(y[0]), "+v"(y[1]), "+v"(y[2]) :: "memory");
    } else asm volatile("s_waitcnt vmcnt(0)" : "+v"(y[0]), "+v"(y[1]), "+v"(y[2]) :: "memory");
#pragma unroll
    for (int i = 0; i < 3; ++i) { const int pc = 64 * i + lane; if (pc < 160) *(LAS v4u*)(lds + L_YT + (20 * tile + (pc >> 3)) * Y_ROW + 16 * (pc & 7)) = y[i]; }
    STAMP(13);
}
#define STARG

__global__ void __launch_bounds__(512, 2) k_fused(Params) {
    u64 tstart;
    asm volatile("s_memrealtime %0" : "=s"(tstart));
    KTouch ka;
    unsigned bx = blockIdx.x;
    const unsigned tid = threadIdx.x;
    ka_touch(ka, bx);
    extern __shared__ __attribute__((aligned(256))) char lds_[];
    LAS char* lds = (LAS char*)lds_;
    const unsigned ldsb = (unsigned)(uintptr_t)lds;
    const int w = __builtin_amdgcn_readfirstlane(tid >> 6), lane = tid & 63, b = bx;
#define KA_READY() const Params p = ka_load(ka); const rsrc_t rws = make_rsrc(p.ws, WS_END);
    unsigned long long pc0;
    asm volatile("s_getpc_b64 %0" : "=s"(pc0));
    const int xcd = b & 7, rr = b >> 3;
    const int r8 = rr >> 2, c4 = rr & 3;
    const int R = 8 * (xcd >> 1) + r8, ct = 4 * (xcd & 1) + c4;
    int rot = ROT == 1 ? r8 + ((5 * c4) >> 1) : ROT == 2 ? 5 * ((r8 ^ c4) & 1) : ROT == 3 ? (r8 >> 1) + ((5 * c4) >> 1) : ROT == 4 ? r8 : ROT == 5 ? 2 * (r8 & 3) + (c4 & 1) : ROT == 6 ? 3 * ((r8 + c4) % 3) :
              ROT == 7 ? (0x8530 >> (4 * ((r8 + c4) & 3))) & 15 : ROT == 8 ? (0x7520 >> (4 * ((r8 + c4) & 3))) & 15 : 0;
    if (ROT >= 9) {
        const int base = (0x7520 >> (4 * ((r8 + c4) & 3))) & 15, pr = xcd >> 1, par = xcd & 1;
        rot = base + (ROT == 9 ? pr : ROT == 10 ? par : ROT == 11 ? par + pr : ROT == 12 ? 3 * pr : ROT == 13 ? 5 * par : ROT == 14 ? pr + 5 * par : 2 * pr);
        if (ROT == 16) rot = ((0x8530 >> (4 * ((r8 + c4) & 3))) & 15) + pr;
        if (ROT == 17) rot = base + 10 - pr;
        if (ROT == 18) rot = 5 * ((r8 + c4) & 1) + pr;
        if (ROT == 19) rot = ((0x7520 >> (4 * ((r8 + c4 + pr) & 3))) & 15) + pr;
        if (ROT == 20) rot = ((0x7520 >> (4 * ((r8 + 2 * c4) & 3))) & 15) + pr;
        if (ROT == 21) rot = ((0x7520 >> (4 * (r8 & 3))) & 15) + pr;
        if (ROT == 22) rot = ((0x7520 >> (4 * c4)) & 15) + pr;
        if (ROT == 23) rot = ((0x6420 >> (4 * ((r8 + c4) & 3))) & 15) + pr;
        if (ROT == 30) rot = 2 * ((c4 + 4 * par + r8) % 5);
        if (ROT == 31) rot = 2 * ((c4 + 4 * par + r8) % 5) + pr;
        if (ROT == 32) rot = 2 * ((c4 + par + r8) % 5);
        if (ROT == 33) rot = 2 * ((c4 + par + r8) % 5) + (pr & 1);
        if (ROT == 34) rot = 2 * ((c4 + par + r8 + pr) % 5);
        if (ROT == 35) rot = 2 * ((c4 + par + r8) % 5) + pr;
        if (ROT == 36) rot = base + pr + 5 * par * (c4 & 1);
        rot = rot >= 20 ? rot - 20 : rot;
    }
    rot = rot >= 10 ? rot - 10 : rot;
    const int j = lane & 15, kq = lane >> 4, q = (lane & 15) >> 2, pq = lane & 3;
    const unsigned fa = ldsb + L_FLAG;

#define EXP_G(x) (x)
#define EXP_L(x) (x)
    if (EXP_G(w < 4)) {
        const int m = w >> 1, n = w & 1;
        __builtin_amdgcn_s_setprio(PRIO_G);
        if (w == 1) {
            unsigned cd;
            asm volatile("buffer_load_dword %0, %1, %2, 0 offen\n\ts_waitcnt vmcnt(0)" : "=&v"(cd)
                         : "v"(lane < 8 ? (rr * 8 + lane) * 128 : OOB), "s"(make_rsrc((const void*)(pc0 & ~127ull), CODE_PF)) : "memory");
        }
        if (w == 0 && lane < 19) *(LAS v4u*)(lds + (lane < 16 ? L_ONES + 16 * lane : L_FLAG + 16 * (lane - 16))) = lane < 16 ? (v4u){0x3f803f80u, 0x3f803f80u, 0x3f803f80u, 0x3f803f80u} : (v4u){0, 0, 0, 0};
        G1Ctx c;
        const int fsw = (j >> 1) & 7;
#pragma unroll
        for (int st = 0; st < 2; ++st)
#pragma unroll
            for (int T = 0; T < 2; ++T) c.xo[st][T] = ldsb + (32 * m + 16 * (T ^ n) + j) * 128 + 16 * ((4 * st + kq) ^ fsw);
        const int sw = ((q >> 1) & 1) | ((kq & 1) << 1);
#pragma unroll
        for (int z = 0; z < 6; ++z) { const int blk = 6 * n + (z + 3 * m) % 6, aa = blk >> 2, bb = blk & 3;
            c.wo[z] = ldsb + aa * 4096 + (8 * kq + q) * 128 + 32 * (bb ^ sw) + 8 * pq; }
        c.ga = ldsb + (j == 1 ? L_GATE + 128 * rot + 16 * kq : j == 0 ? L_ONES : L_ZERO);
        c.ginc = j == 1 ? 256 : 0;
#pragma unroll
        for (int z = 0; z < 6; ++z) { c.acc[z][0] = c.acc[z][1] = (f32x4){0.f, 0.f, 0.f, 0.f}; }
        c.st = c.gr = c.cs[0] = c.cs[1] = c.cs[2] = (f32x4){0.f, 0.f, 0.f, 0.f};
        c.ones = (v4u){0x3f803f80u, 0x3f803f80u, 0x3f803f80u, 0x3f803f80u};
        PIN(c.ones);
        KA_READY();
        u64 tag = load_tag(p.ws);
        unsigned a_st = ldsb + L_STATS + 8 * (16 * w + j), a_gt = ldsb + L_GT + 4 * (16 * w + j), a_cs = ldsb + L_CS + ((6 * n + 3 * m) * 16 + 4 * kq) * 4;
        unsigned a_cso = ldsb + L_CS + ((6 * n + 3 * (1 - m)) * 16 + 4 * kq) * 4, a_st1 = ldsb + L_STATS + 8 * (16 * (w ^ 1) + j), l16 = lane * 16;
        unsigned ho0 = WS_H + ((4 * R + 2 * m + n) * 48 + 6 * ct + 3 * n) * 1024 + l16, ho1 = WS_H + ((4 * R + 2 * m + (n ^ 1)) * 48 + 6 * ct + 3 * n) * 1024 + l16;
        unsigned a_b1 = ldsb + L_B1 + (96 * n + 48 * m + 4 * kq) * 2, a_b1o = ldsb + L_B1 + (96 * n + 48 * (1 - m) + 4 * kq) * 2;
        PIN(a_st); PIN(a_gt); PIN(a_cs); PIN(a_cso); PIN(a_st1); PIN(l16); PIN(ho0); PIN(ho1); PIN(a_b1); PIN(a_b1o);
        G2Ctx g;
        g.rh = make_rsrc(p.ws + WS_H + ((size_t)(4 * R) * 48 + 12 * w) * 1024, 4 * 49152);
        g.rw = make_rsrc(p.ws + WS_W2T + (size_t)(ct * 48 + 12 * w) * G2_PIECE, 12 * G2_PIECE);
        rsrc_t rwsp = rws;
        asm volatile("" : "+s"(tag), "+s"(g.rh), "+s"(g.rw), "+s"(rwsp));
        STAMP(1);
        wg_barrier();
        STAMP(2);
        CSTAMP(15);
        g1_reads<0>(c);
        f32x4 b1f[6];
#pragma unroll
        for (int z = 0; z < 6; ++z) {
            const v2u bv = lds_ld64v(la(z < 3 ? a_b1 : a_b1o) + 32 * (z % 3));
            b1f[z] = (f32x4){bf_lo(bv.x), bf_hi(bv.x), bf_lo(bv.y), bf_hi(bv.y)};
        }
#pragma unroll
        for (int z = 0; z < 6; ++z) PIN(b1f[z]);
        SCHED_FENCE();
#pragma unroll 1
        for (int pr = 0; pr < 5; ++pr) {
            g1_block<0>(c, 0); g1_block<1>(c, 0); g1_block<2>(c, pr >> 2); g1_block<3>(c, 0);
        }
        STAMP(3);
        CSTAMP(16);
        f32x2 st0;
        {
            const bool j1 = __builtin_amdgcn_inverse_ballot_w64(0xAAAAAAAAAAAAAAAAull), j2 = __builtin_amdgcn_inverse_ballot_w64(0xCCCCCCCCCCCCCCCCull),
                       dgl = __builtin_amdgcn_inverse_ballot_w64(0xF0000F0000F0000Full);
            const float g01 = j1 ? c.gr[1] : c.gr[0], g23 = j1 ? c.gr[3] : c.gr[2], dg = j2 ? g23 : g01;
            const float s1 = sum_rows(c.st[0]), s2 = sum_rows(dgl ? dg : 0.f), gd = sum_rows(c.st[1]);
            const float mean = s1 * (1.0f / D_), var = fmaxf(s2 * (1.0f / D_) - mean * mean, 0.f), rstd = __builtin_amdgcn_rsqf(var + 1e-5f);
            const float gate = __builtin_amdgcn_rcpf(1.0f + __builtin_amdgcn_exp2f(-1.4426950408889634f * gd));
            st0 = (f32x2){rstd, -mean * rstd};
            u64 sv;
            asm volatile("s_mov_b64 %0, exec\n\ts_mov_b64 exec, 0xffff\n\tds_write_b64 %1, %2\n\tds_write_b32 %3, %4\n\t"
                         "s_mov_b64 exec, %9\n\tds_write_b128 %5, %6\n\tds_write_b128 %5, %7 offset:64\n\tds_write_b128 %5, %8 offset:128\n\ts_mov_b64 exec, %0"
                         : "=&s"(sv) : "v"(a_st), "v"(st0), "v"(a_gt), "v"(gate), "v"(a_cs), "v"(c.cs[0]), "v"(c.cs[1]), "v"(c.cs[2]), "s"(0x0001000100010001ull) : "memory");
        }
        lf_signal<F_CG>(fa);
        STAMP(4);
        __builtin_amdgcn_s_setprio(PRIO_E);
        {
            v2u o[6][2];
            auto act = [&](int z, int T, f32x2 st, f32x4 cs) {
                const f32x2 b01 = {b1f[z][0], b1f[z][1]}, b23 = {b1f[z][2], b1f[z][3]};
                const f32x2 a01 = {c.acc[z][T][0], c.acc[z][T][1]}, a23 = {c.acc[z][T][2], c.acc[z][T][3]};
                const f32x2 c01 = {cs[0], cs[1]}, c23 = {cs[2], cs[3]};
                o[z][T] = (v2u){pk_bf16v(gelu2(a01 * st.x + (c01 * st.y + b01))), pk_bf16v(gelu2(a23 * st.x + (c23 * st.y + b23)))};
            };
            auto put = [&](int T) {
#pragma unroll
                for (int zp = 0; zp < 3; ++zp) {
                    const v2u lo = m ? o[(2 * zp + 3) % 6][T] : o[2 * zp][T], hi = m ? o[(2 * zp + 4) % 6][T] : o[2 * zp + 1][T];
                    __builtin_amdgcn_raw_buffer_store_b128((v4u){lo.x, lo.y, hi.x, hi.y}, rwsp, (T ? ho1 : ho0) + zp * 1024, 0, H_AUX);
                }
            };
#pragma unroll
            for (int z = 0; z < 3; ++z) act(z, 0, st0, c.cs[z]);
#pragma unroll
            for (int z = 0; z < 3; ++z) PIN(o[z][0]);
            lf_wait<F_CG>(fa, 4);
            f32x4 csf[3];
#pragma unroll
            for (int z = 0; z < 3; ++z) csf[z] = *(LAS f32x4*)(la(a_cso) + 64 * z);
            const f32x2 st1 = *(LAS f32x2*)la(a_st1);
#pragma unroll
            for (int z = 0; z < 3; ++z) act(3 + z, 0, st0, csf[z]);
            put(0);
#pragma unroll
            for (int z = 0; z < 6; ++z) act(z, 1, st1, z < 3 ? c.cs[z] : csf[z - 3]);
            put(1);
        }
        lf_signal<F_HS>(fa);
        STAMP(5);
        if (PRIO_GL != PRIO_E) __builtin_amdgcn_s_setprio(PRIO_GL);
        SCHED_FENCE();
#pragma unroll
        for (int T = 0; T < 4; ++T) g.hv[T] = l16 + ((w + T) & 3) * 49152;
        g.wv = l16; g.fa = fa; g.rot = G2ROT * c4;
        g.lw = lds + w * G2_PIECE + lane * 16;
        lf_wait<F_W2OK>(fa, 1);
        if constexpr (WPRE > 0) g2_issue_w<0>(g);
        if constexpr (WPRE > 1) g2_issue_w<1>(g);
        if constexpr (WPRE > 2) g2_issue_w<2>(g);
        if constexpr (WPRE > 3) g2_issue_w<3>(g);
        if constexpr (WPRE > 4 && NS == 5) g2_issue_w<4>(g);
        wait_vm<5 * (WPRE < NS ? WPRE : NS)>();
        publish_last<F_HJ, 4>(fa, rwsp, WS_FH + (R * 8 + ct) * 128, tag);
        STAMP(6);
        if constexpr (WPRE <= 0) g2_issue_w<0>(g);
        if constexpr (WPRE <= 1) g2_issue_w<1>(g);
        if constexpr (WPRE <= 2) g2_issue_w<2>(g);
        if constexpr (WPRE <= 3) g2_issue_w<3>(g);
        if constexpr (WPRE <= 4 && NS == 5) g2_issue_w<4>(g);
        if (DG_GATE) lf_wait<F_CW>(fa, 3);
#pragma unroll
        for (int s = 8; s < 12; ++s) dma5_sc1(g.rw, ldsb + g2_slot(s - 8) + w * G2_PIECE, l16, s * G2_PIECE, s * G2_PIECE + 4096);
#pragma unroll
        for (int t5 = 0; t5 < 5; ++t5)
#pragma unroll
            for (int T = 0; T < 4; ++T) g.acc[t5][T] = (f32x4){0.f, 0.f, 0.f, 0.f};
        lf_wait<F_B2>(fa, 1);
#pragma unroll
        for (int t5 = 0; t5 < 5; ++t5) { g.acc[t5][0] = *(LAS f32x4*)(lds + L_B2F + t5 * 64 + kq * 16); PIN(g.acc[t5][0]); }
        g.ldsb = ldsb; g.w = w; g.tok0 = 64 * R + 16 * w; g.col0 = 160 * ct;
        bool seen = false;
        if (HINT_H) seen = hint2(p.ws + WS_FH + (R * 8 + 2 * w) * 128, tag);
        CSTAMP(22);
        if (!(SKIPV && seen)) poll_tags(rws, WS_FH + (R * 8 + 2 * w + (lane & 1)) * 128, tag);
        STAMP(7);
        auto h_first = [&]() {
            if constexpr (HV1) {
#pragma unroll
                for (int T = 0; T < 4; ++T) g.ht[T] = ((w + T) & 3) * 49152;
#pragma unroll
                for (int s = 0; s < 4; ++s)
#pragma unroll
                    for (int T = 0; T < 4; ++T) ld128_sc1<0>(g.h[s][T], g.rh, g.wv, g.ht[T] + s * 1024);
            } else { g2_issue_h<0>(g); g2_issue_h<1>(g); g2_issue_h<2>(g); g2_issue_h<3>(g); }
        };
        if (H_FIRST) h_first();
        if (PRE_H > 0) g2_issue_h<0>(g);
        if (PRE_H > 1) g2_issue_h<1>(g);
        if (H_FIRST && PRIO_GL != PRIO_G2) __builtin_amdgcn_s_setprio(PRIO_G2);
        if (YS_N) lf_wait_n<F_YS>(fa, 3, w == 3 && YS_W3 >= 0 ? (YS_W3 > 0 ? YS_W3 : 1) : YS_N);
        if (!H_FIRST) { h_first(); if (PRIO_GL != PRIO_G2) __builtin_amdgcn_s_setprio(PRIO_G2); }
        SCHED_FENCE();
        g2n_steps(g, mkseq<12>::type{});
        STAMP(8);
        rsrc_t ro = make_rsrc(p.out, S_ * D_ * 2);
        asm volatile("" : "+s"(ro));
        STAMP(9);
        wg_barrier();
        STAMP(10);
        f32x4 pt[3][5], gq[5]; v2u yv[5];
#pragma unroll
        for (int t5 = 0; t5 < 5; ++t5) {
#pragma unroll
            for (int T = 1; T < 4; ++T) pt[T - 1][t5] = *(LAS f32x4*)(la(g.a_src[T - 1]) + t5 * 1024);
            gq[t5] = *(LAS f32x4*)(la(g.a_fin) + t5 * 1024); yv[t5] = tr_read(la(g.a_y) + 16 * t5 * Y_ROW);
        }
        const float omg = 1.0f - g.omg;
        const unsigned oo0 = g.oo0, oo4 = g.oo4;
        unsigned o[5][2];
#pragma unroll
        for (int t5 = 0; t5 < 5; ++t5) {
            const f32x4 sacc = ((g.acc[t5][0] + pt[0][t5]) + (pt[1][t5] + pt[2][t5])) * omg;
            const f32x4 r = gq[t5] * (f32x4){bf_lo(yv[t5].x), bf_hi(yv[t5].x), bf_lo(yv[t5].y), bf_hi(yv[t5].y)} + sacc;
            o[t5][0] = pk_bf16(r[0], r[1]); o[t5][1] = pk_bf16(r[2], r[3]);
        }
#pragma unroll
        for (int t5 = 0; t5 < 4; t5 += 2) {
            const auto x = __builtin_amdgcn_permlane16_swap(o[t5][0], o[t5 + 1][0], false, false);
            const auto y = __builtin_amdgcn_permlane16_swap(o[t5][1], o[t5 + 1][1], false, false);
            __builtin_amdgcn_raw_buffer_store_b128((v4u){x[0], y[0], x[1], y[1]}, ro, oo0 + 32 * t5, 0, OUT_AUX);
        }
        __builtin_amdgcn_raw_buffer_store_b64((v2u){o[4][0], o[4][1]}, ro, oo4, 0, OUT_AUX);
        STAMP(11); CSTAMP(31); STFLUSH();
    } else if (EXP_L(w == 7)) {
        __builtin_amdgcn_s_setprio(PRIO_L);
        G1Ld c;
        {
            const int row = lane >> 3, pp = lane & 7;
#pragma unroll
            for (int i = 0; i < 4; ++i) {
                c.xv[i] = (64 * R + 8 * i + row) * (D_ * 2) + 16 * (pp ^ (row >> 1) ^ (4 * (i & 1))) + BIAS - 1024 * i;
                c.wv[0][i] = (8 * i + row) * (F_ * 2) + 16 * (pp ^ (2 * (((row >> 1) & 1) | ((i & 1) << 1)))) + BIAS - 1024 * i;
                c.wv[1][i] = c.wv[0][i] + 128; c.wv[2][i] = c.wv[0][i] + 256;
            }
        }
        unsigned fwo = WS_FW + (lane < 48 ? (lane % 24) * 5 + ((5 * ct + (lane >= 24 ? 4 : 0)) >> 3) : (5 * ct) >> 3) * FW_ST;
        unsigned tv = lane < 60 ? r8 * 30720 + lane * 64 : OOB;
        PIN(fwo); PIN(tv);
        c.sx = 128 * rot; c.sw = 384 * ct + 64 * (F_ * 2) * rot;
#pragma unroll
        for (int i = 0; i < 4; ++i) { PIN(c.xv[i]); PIN(c.wv[0][i]); PIN(c.wv[1][i]); PIN(c.wv[2][i]); }
        asm volatile("" : "+s"(c.sx), "+s"(c.sw));
        KA_READY();
        c.rx = make_rsrc_biased(p.q, S_ * D_ * 2); c.rw = make_rsrc_biased(p.w1, D_ * F_ * 2);
        if (DLY_L) __builtin_amdgcn_s_sleep(DLY_L);
        {
            u64 t0;
            asm volatile("s_mov_b64 %0, %1" : "=s"(t0) : "s"(tstart));
#pragma unroll 1
            for (int n = 0; n < 1000 && (unsigned)__builtin_amdgcn_s_memrealtime() - (unsigned)t0 < DLY_T; ++n) __builtin_amdgcn_s_sleep(1);
        }
        STAMP(1);
        g1_chunk<0, false>(c, ldsb);
        const u64 epoch = load_epoch(p.ws);
        {
            const rsrc_t rg = make_rsrc(p.gw, D_ * 2), rb = make_rsrc(p.b1, F_ * 2);
            dma1(rg, ldsb + L_GATE, lane * 16, 0);
            dma1(rg, ldsb + L_GATE + 1024, lane < 16 ? 1024 + lane * 16 : lane * 16 - 256, 0);
            dma1(rg, ldsb + L_GATE + 2048, lane < 32 ? 768 + lane * 16 : OOB, 0);
            dma1(rb, ldsb + L_B1, lane < 24 ? 384 * ct + lane * 16 : OOB, 0);
        }
        g1_chunk<1, false>(c, ldsb);
        g1_chunk<2, true>(c, ldsb); LSTAMP(16);
        g1_chunk<3, true>(c, ldsb); g1_chunk<4, true>(c, ldsb); LSTAMP(17);
        g1_chunk<5, true>(c, ldsb); g1_chunk<6, true>(c, ldsb); LSTAMP(18);
        g1_chunk<7, true>(c, ldsb); g1_chunk<8, true>(c, ldsb); LSTAMP(19);
        const rsrc_t rw = make_rsrc(p.ws + WS_W2T + (size_t)ct * 48 * G2_PIECE, 48 * G2_PIECE);
        auto touch = [&](unsigned& dm, unsigned vo) {
            unsigned so;
            asm volatile("s_mov_b32 %1, 0\n\t"
                         "buffer_load_dword %0, %2, %3, %1 offen sc1\n\tbuffer_load_dword %0, %2, %3, %1 offen offset:3840 sc1\n\ts_add_u32 %1, %1, 7680\n\t"
                         "buffer_load_dword %0, %2, %3, %1 offen sc1\n\tbuffer_load_dword %0, %2, %3, %1 offen offset:3840 sc1\n\ts_add_u32 %1, %1, 7680\n\t"
                         "buffer_load_dword %0, %2, %3, %1 offen sc1\n\tbuffer_load_dword %0, %2, %3, %1 offen offset:3840 sc1\n\ts_add_u32 %1, %1, 7680\n\t"
                         "buffer_load_dword %0, %2, %3, %1 offen sc1\n\tbuffer_load_dword %0, %2, %3, %1 offen offset:3840 sc1"
                         : "=&v"(dm), "=&s"(so) : "v"(vo), "s"(rw) : "memory", "scc");
        };
        g1_chunk<9, true>(c, ldsb);
        STAMP(2);
        wait_vm<32>(); wg_barrier();
        STAMP(20);
        wait_vm<0>(); wg_barrier();
        STAMP(21);
        const u64 tag = tag_ready(epoch);
        poll_tags_slow<FW_SLP>(rws, fwo, tag);
        lf_signal<F_W2OK>(fa);
        FinQ fq; final_loads<true>(fq, p, R, ct, 3, lane);
        v4u wr[2][4][5];
        auto wl_issue = [&]() {
#pragma unroll
            for (int s = 0; s < 2; ++s)
#pragma unroll
                for (int ww = 0; ww < 4; ++ww) {
                    const unsigned so = (12 * ww + WL0 + s) * G2_PIECE;
                    ld128_sc1<0>(wr[s][ww][0], rw, lane * 16, so); ld128_sc1<1024>(wr[s][ww][1], rw, lane * 16, so);
                    ld128_sc1<2048>(wr[s][ww][2], rw, lane * 16, so); ld128_sc1<3072>(wr[s][ww][3], rw, lane * 16, so);
                    ld128_sc1<0>(wr[s][ww][4], rw, lane * 16, so + 4096);
                }
        };
        auto wl_landed = [&]() {
#pragma unroll
            for (int s = 0; s < 2; ++s)
#pragma unroll
                for (int ww = 0; ww < 4; ++ww)
                    asm volatile("" : "+v"(wr[s][ww][0]), "+v"(wr[s][ww][1]), "+v"(wr[s][ww][2]), "+v"(wr[s][ww][3]), "+v"(wr[s][ww][4]) :: "memory");
        };
        {
            unsigned dummy;
            touch(dummy, tv);
            auto b2_put = [&]() {
                final_landed<true>(fq);
                if ((lane & 15) == 0) {
#pragma unroll
                    for (int t5 = 0; t5 < 5; ++t5)
                        *(LAS f32x4*)(lds + L_B2F + t5 * 64 + (lane >> 4) * 16) = (f32x4){bf_lo(fq.b[t5].x), bf_hi(fq.b[t5].x), bf_lo(fq.b[t5].y), bf_hi(fq.b[t5].y)};
                }
                lf_arrive<F_B2>(fa);
            };
            __builtin_amdgcn_s_setprio(PRIO_L1);
            STAMP(3);
            __builtin_amdgcn_s_setprio(0);
            asm volatile("s_waitcnt vmcnt(%1)" : "+v"(dummy) : "i"((W45 && WL_EARLY == 1) ? 40 : 0) : "memory");
            b2_put();
            lf_wait_slow<LD_GATE>(fa, 4); if (CW_GATE) lf_wait_slow<F_CW>(fa, 3);
            wl_issue();
        }
        __builtin_amdgcn_s_setprio(PRIO_L2);
        STAMP(4);
        {
#pragma unroll
            for (int s = 8; s < ((G2N && DMA_G) ? 8 : 12); ++s) {
                int k = s + G2ROT * c4; k = k >= 12 ? k - 12 : k;
#pragma unroll 1
                for (int ww = 0; ww < 4; ++ww) {
                    const unsigned so = (12 * ww + k) * G2_PIECE;
                    dma5_sc1(rw, ldsb + g2_slot(s - 8) + ww * G2_PIECE, lane * 16, so, so + 4096);
                }
            }
            STAMP(5);
            auto wl_put = [&](int s) {
#pragma unroll
                for (int ww = 0; ww < 4; ++ww)
#pragma unroll
                    for (int t5 = 0; t5 < 5; ++t5) *(LAS v4u*)(lds + L_W45 + s * G2_SLOT + ww * G2_PIECE + t5 * 1024 + lane * 16) = wr[s][ww][t5];
            };
            if (DUMP_FIRST && !DMA_G) wait_vm<56>(); else wait_vm<0>();
            wl_landed();
            STAMP(6);
            lf_wait<CE_EARLY ? F_CE : F_YS>(fa, 3);
            __builtin_amdgcn_s_setprio(PRIO_L3);
            wl_put(0); lf_signal<F_W45>(fa);
            wl_put(1); lf_signal<F_W45>(fa);
            if (!DMA_G) { wait_vm<0>(); lf_signal<F_W45>(fa); }
        }
        if (b == 0) {
            v4u el[5];
            final_prep(p, rws, fa, lds, fq, tag, R, ct, 3, lane, el STARG);
            wg_barrier();
            asm volatile("s_waitcnt vmcnt(0)" : "+v"(el[0]), "+v"(el[1]), "+v"(el[2]), "+v"(el[3]), "+v"(el[4]) :: "memory");
            const unsigned tlo = (unsigned)tag, thi = (unsigned)(tag >> 32);
#pragma unroll
            for (int i = 0; i < 5; ++i)
                if (__builtin_amdgcn_ballot_w64((((el[i].x ^ tlo) | (el[i].y ^ thi)) | ((el[i].z ^ tlo) | (el[i].w ^ thi))) != 0)) {
                    poll_tags(rws, WS_FY + 1024 * i + 16 * lane, tag);
                    poll_tags(rws, WS_FY + 1024 * i + 16 * lane + 8, tag);
                }
            publish(rws, WS_E, tag);
        } else {
            final_prep(p, rws, fa, lds, fq, tag, R, ct, 3, lane, nullptr STARG);
            wg_barrier();
        }
        STFLUSH();
    } else {
        const int a = w - 4;
        const bool has = a < (b < 128 ? 3 : 2);
        const int task = task_of(b, a);
        __builtin_amdgcn_s_setprio(PRIO_W);
        const int cht = task % 10, tt = task / 10, cg = lane & 15, tg = lane >> 4;
        const int pch = 64 * cht + 4 * cg, tb = 32 * tt + 8 * tg;
        unsigned off0 = has ? tb * (D_ * 2) + pch * 2 : OOB;
        unsigned offh = (has && tb > 0) ? off0 - 3 * (D_ * 2) : OOB;
        unsigned off1 = off0 + 4 * (D_ * 2), offs = has ? pch * 8 : OOB;
        unsigned ko = has ? WS_KVR + pch * (S_ * 2) + (2040 - tb) * 2 : OOB;
        PIN(off0); PIN(offh); PIN(off1); PIN(offs); PIN(ko);
        KA_READY();
        const u64 epoch = load_epoch(p.ws);
        u64 tag;
        if (DLY_W) __builtin_amdgcn_s_sleep(DLY_W);
        v4u hw[4]; unsigned dv = 0;
        FinQ fq; v2u fl;
        v4u sh[4][3], ez;
        const unsigned fo = has ? WS_FKV + ((task >> 6) * 64 + lane) * FKV_ST : OOB;
        auto cp_piece = [&](auto P) {
                constexpr int i = decltype(P)::v;
                pin4(hw[i]);
                unsigned ox = 0, oy = 0;
                if constexpr (i < 3) { ox = __builtin_amdgcn_readfirstlane(hw[i + 1].x); oy = __builtin_amdgcn_readfirstlane(hw[i + 1].y); }
                const unsigned aa[6] = {hw[i].x, hw[i].y, hw[i].z, hw[i].w, (unsigned)__builtin_amdgcn_update_dpp((int)ox, (int)hw[i].x, 0x130, 0xf, 0xf, false),
                                        (unsigned)__builtin_amdgcn_update_dpp((int)oy, (int)hw[i].y, 0x130, 0xf, 0xf, false)};
                sh[i][0] = (v4u){__builtin_amdgcn_alignbit(aa[1], aa[0], 16), __builtin_amdgcn_alignbit(aa[2], aa[1], 16), __builtin_amdgcn_alignbit(aa[3], aa[2], 16), __builtin_amdgcn_alignbit(aa[4], aa[3], 16)};
                sh[i][1] = (v4u){aa[1], aa[2], aa[3], aa[4]};
                sh[i][2] = (v4u){__builtin_amdgcn_alignbit(aa[2], aa[1], 16), __builtin_amdgcn_alignbit(aa[3], aa[2], 16), __builtin_amdgcn_alignbit(aa[4], aa[3], 16), __builtin_amdgcn_alignbit(aa[5], aa[4], 16)};
                pin4(sh[i][0]); pin4(sh[i][1]); pin4(sh[i][2]);
            };
        auto cp_edge = [&]() {
                    pin4(hw[0]);
                    const unsigned h0x = __builtin_amdgcn_readfirstlane(hw[0].x), h0y = __builtin_amdgcn_readfirstlane(hw[0].y);
                    const int rho = lane >> 1;
                    ez = (v4u){0, 0, 0, 0};
                    if (lane & 1) {
                        if (rho == 1) ez.w = h0x << 16;
                        if (rho == 2) ez.w = h0x;
                        if (rho == 3) { ez.z = h0x << 16; ez.w = __builtin_amdgcn_alignbit(h0y, h0x, 16); }
                    }
                    pin4(ez);
                };
        if (a == 2 && b >= 128) {
            const bool rel = b < 248;
            const int rks = (b - 128) / 5, rcg = (b - 128) % 5;
            const rsrc_t rw2 = make_rsrc(p.w2, F_ * D_ * 2);
            const int srow = lane >> 3;
            const unsigned sv = rel ? (64 * rks + srow) * (D_ * 2) + 256 * rcg + 16 * ((lane & 7) ^ (2 * ((srow >> 1) & 3))) : OOB;
            const unsigned so = L_STG + (4 * kq + q) * 128 + 8 * pq;
            auto frags = [&](int blk) {
                v4u fr[4];
#pragma unroll
                for (int t = 0; t < 4; ++t) fr[t] = tr_read2(lds + so + 4096 * blk + 32 * (t ^ ((q >> 1) | ((kq & 1) << 1))), 2048);
#pragma unroll
                for (int t = 0; t < 4; ++t) { const int gt = 8 * rcg + 4 * (blk & 1) + t;
                    __builtin_amdgcn_raw_buffer_store_b128(fr[t], rws, rel ? WS_W2T + (((gt / 5) * 48 + 2 * rks + (blk >> 1)) * 5 + gt % 5) * 1024 + lane * 16 : OOB, 0, RL_AUX); }
            };
            auto iv = [&](auto I) {
                constexpr int i = decltype(I)::v;
                if constexpr (i == POS_RA) {
#pragma unroll
                    for (int blk = 0; blk < 4; ++blk)
#pragma unroll
                        for (int g = 0; g < 4; ++g) (R_NT ? dma1_nt : dma1)(rw2, ldsb + L_STG + 4096 * blk + 1024 * g, sv, (32 * (blk >> 1) + 8 * g) * (D_ * 2) + 128 * (blk & 1));
                }
                if constexpr (i == POS_RB) wait_vm<0>();
                if constexpr (i == POS_RB + 0 / R_PER) frags(0);
                if constexpr (i == POS_RB + 1 / R_PER) frags(1);
                if constexpr (i == POS_RB + 2 / R_PER) frags(2);
                if constexpr (i == POS_RB + 3 / R_PER) frags(3);
                if constexpr (i == POS_RP) { wait_vm<0>(); tag = tag_ready(epoch); if (rel) publish(rws, WS_FW + (b - 128) * FW_ST, tag); }
                wg_barrier();
            };
            STAMP(1);
            for_intervals(iv);
        } else {
            v2u kk[11], vv[8]; v4u sA, sB;
            const rsrc_t rk = make_rsrc(p.k, S_ * D_ * 2), rv = make_rsrc(p.v, S_ * D_ * 2), rs = make_rsrc(p.shift, D_ * 8), rh = make_rsrc(p.ssm, S_ * D_ * 2);
            unsigned keep[4];
            v4u kvo[4];
            auto kv_piece = [&](auto P) {
                constexpr int pc = decltype(P)::v, cp = pc >> 1, t0 = (pc & 1) ? 0 : 4;
                v4u& sv = cp ? sB : sA;
                asm volatile("" : "+v"(kk[t0]), "+v"(kk[t0 + 1]), "+v"(kk[t0 + 2]), "+v"(kk[t0 + 3]), "+v"(kk[t0 + 4]), "+v"(kk[t0 + 5]), "+v"(kk[t0 + 6]),
                                  "+v"(vv[t0]), "+v"(vv[t0 + 1]), "+v"(vv[t0 + 2]), "+v"(vv[t0 + 3]), "+v"(sv));
                f32x2 kf[7], kv[4];
#pragma unroll
                for (int i = 0; i < 7; ++i) { const unsigned u = cp ? kk[t0 + i].y : kk[t0 + i].x; kf[i] = (f32x2){bf_lo(u), bf_hi(u)}; }
                const f32x2 s0 = {bf_lo(sv.x), bf_lo(sv.z)}, s1 = {bf_hi(sv.x), bf_hi(sv.z)}, s2 = {bf_lo(sv.y), bf_lo(sv.w)}, s3 = {bf_hi(sv.y), bf_hi(sv.w)};
#pragma unroll
                for (int t = 0; t < 4; ++t) { const unsigned u = cp ? vv[t0 + t].y : vv[t0 + t].x;
                    kv[t] = (s0 * kf[t + 3] + s1 * kf[t + 2] + s2 * kf[t + 1] + s3 * kf[t]) * (f32x2){bf_lo(u), bf_hi(u)}; }
                const unsigned x0 = pk_bf16(kv[3].x, kv[2].x), x1 = pk_bf16(kv[1].x, kv[0].x), y0 = pk_bf16(kv[3].y, kv[2].y), y1 = pk_bf16(kv[1].y, kv[0].y);
                if constexpr ((pc & 1) == 0) {
                    keep[0] = x0; keep[1] = x1; keep[2] = y0; keep[3] = y1;
                    asm volatile("" : "+v"(keep[0]), "+v"(keep[1]), "+v"(keep[2]), "+v"(keep[3]));
                } else {
                    kvo[2 * cp] = (v4u){keep[0], keep[1], x0, x1}; kvo[2 * cp + 1] = (v4u){keep[2], keep[3], y0, y1};
                    if constexpr (POS_ST <= POS_KV0) {
                        __builtin_amdgcn_raw_buffer_store_b128(kvo[2 * cp], rws, ko + (2 * cp) * KV_ROW, 0, KV_AUX);
                        __builtin_amdgcn_raw_buffer_store_b128(kvo[2 * cp + 1], rws, ko + (2 * cp + 1) * KV_ROW, 0, KV_AUX);
                    } else { pin4(kvo[2 * cp]); pin4(kvo[2 * cp + 1]); }
                }
            };
            auto iv = [&](auto I) {
                constexpr int i = decltype(I)::v;
                if constexpr (i == POS_K) {
                    ld64_nt(kk[0], rk, offh, 0); ld64_nt(kk[1], rk, offh, 1280); ld64_nt(kk[2], rk, offh, 2560);
                    ld64_nt(kk[3], rk, off0, 0); ld64_nt(kk[4], rk, off0, 1280); ld64_nt(kk[5], rk, off0, 2560); ld64_nt(kk[6], rk, off0, 3840);
                    ld64_nt(kk[7], rk, off1, 0); ld64_nt(kk[8], rk, off1, 1280); ld64_nt(kk[9], rk, off1, 2560); ld64_nt(kk[10], rk, off1, 3840);
                    ld128(sA, rs, offs, 0); ld128(sB, rs, offs, 16);
                    asm volatile("buffer_load_ushort %0, %1, %2, 0 offen" : "=v"(dv) : "v"(has ? task * 2 : OOB), "s"(make_rsrc(p.ssmD, D_ * 2)) : "memory");
                }
                if constexpr (i == POS_V) {
                    ld64_nt(vv[0], rv, off0, 0); ld64_nt(vv[1], rv, off0, 1280); ld64_nt(vv[2], rv, off0, 2560); ld64_nt(vv[3], rv, off0, 3840);
                    ld64_nt(vv[4], rv, off1, 0); ld64_nt(vv[5], rv, off1, 1280); ld64_nt(vv[6], rv, off1, 2560); ld64_nt(vv[7], rv, off1, 3840);
                }
                if constexpr (i == POS_KV0) {
                    asm volatile("s_waitcnt vmcnt(%c[n])" : "+v"(kk[0]), "+v"(kk[1]), "+v"(kk[2]), "+v"(kk[3]), "+v"(kk[4]), "+v"(kk[5]), "+v"(kk[6]), "+v"(kk[7]), "+v"(kk[8]), "+v"(kk[9]), "+v"(kk[10]),
                                 "+v"(vv[0]), "+v"(vv[1]), "+v"(vv[2]), "+v"(vv[3]), "+v"(vv[4]), "+v"(vv[5]), "+v"(vv[6]), "+v"(vv[7]), "+v"(sA), "+v"(sB), "+v"(dv)
                                 : [n] "i"((Y_SENT && POS_YF == POS_V && POS_V == POS_KV0) ? 4 : 0) : "memory");
                    if constexpr (POS_H <= POS_KV0) asm volatile("" : "+v"(hw[0]), "+v"(hw[1]), "+v"(hw[2]), "+v"(hw[3]) :: "memory");
                    STAMP(2);
                }
                if constexpr (i == POS_KV0 + 0 / KV_PER) kv_piece(ic<0>{});
                if constexpr (i == POS_KV0 + 1 / KV_PER) kv_piece(ic<1>{});
                if constexpr (i == POS_KV0 + 2 / KV_PER) kv_piece(ic<2>{});
                if constexpr (i == POS_KV0 + 3 / KV_PER) { kv_piece(ic<3>{}); STAMP(3); }
                if constexpr (i == POS_ST && POS_ST > POS_KV0) {
#pragma unroll
                    for (int r = 0; r < 4; ++r) { pin4(kvo[r]); __builtin_amdgcn_raw_buffer_store_b128(kvo[r], rws, ko + r * KV_ROW, 0, AUX_SC1); }
                }
                if constexpr (i == POS_H && !F_DMA) {
                    const unsigned ho = has ? task * 4096 + 16 * lane : OOB;
                    ld128_nt(hw[0], rh, ho, 0); ld128_nt(hw[1], rh, ho, 1024); ld128_nt(hw[2], rh, ho, 2048); ld128_nt(hw[3], rh, ho, 3072);
                }
                if constexpr (i == POS_PUB) {
                    wait_vm<(POS_H >= POS_KV0 + 3 / KV_PER && POS_H <= POS_PUB) ? 4 : 0>();
                    tag = tag_ready(epoch);
                    if (has) publish(rws, WS_FKV + (cht * 64 + tt) * FKV_ST, tag);
                    STAMP(4);
                }
                if constexpr (i == POS_CP) { cp_piece(ic<0>{}); cp_piece(ic<1>{}); }
                if constexpr (i == POS_CP + 1) { cp_piece(ic<2>{}); cp_piece(ic<3>{}); }
                if constexpr (i == POS_CP + 2) cp_edge();
                if constexpr (i == 8) {
                    look_tags(fl, rws, fo);
                }
                SCHED_FENCE();
                wg_barrier();
            };
            STAMP(1);
            for_intervals(iv);
        }
        __builtin_amdgcn_s_setprio(PRIO_C);
        LAS char* reg = lds + L_CV + a * CV_REG;
        if (!has) { final_loads<false>(fq, p, R, ct, a, lane); if (KI_GATE && WL_EARLY == 1) lf_signal<F_KI>(fa); wait_vm<0>(); final_landed<false>(fq); }
        if (has) {
            if constexpr (POS_H > POS_KV0) asm volatile("s_waitcnt vmcnt(0)" : "+v"(fl), "+v"(hw[0]), "+v"(hw[1]), "+v"(hw[2]), "+v"(hw[3]) :: "memory");
            else asm volatile("s_waitcnt vmcnt(0)" : "+v"(fl) :: "memory");
            if (__builtin_amdgcn_ballot_w64((((u64)fl.y << 32) | fl.x) != tag)) poll_tags_slow<KV_SLP>(rws, fo, tag);
            STAMP(6);
            v4u xr[4];
#pragma unroll
            for (int i = 0; i < 4; ++i) {
                ld128_sc1<0>(xr[i], rws, WS_KVR + task * 4096 + 16 * lane, 1024 * i);
            }
            final_loads<false>(fq, p, R, ct, a, lane);
            if (KI_GATE && WL_EARLY == 1) lf_signal<F_KI>(fa);
            if constexpr (POS_CP > 8) { cp_piece(ic<0>{}); cp_piece(ic<1>{}); cp_piece(ic<2>{}); cp_piece(ic<3>{}); cp_edge(); }
#pragma unroll
            for (int i = 0; i < 4; ++i) {
                LAS char* dst = reg + 32 + 1024 * i + 16 * lane;
                *(LAS v4u*)dst = hw[i];
#pragma unroll
                for (int r = 0; r < 3; ++r) *(LAS v4u*)(dst + (r + 1) * CV_COPY) = sh[i][r];
            }
            if (lane < 8) *(LAS v4u*)(reg + (lane >> 1) * CV_COPY + 16 * (lane & 1)) = ez;
            if (lane < 30) *(LAS v4u*)(reg + CV_XR + 4096 + 16 * lane) = (v4u){0, 0, 0, 0};
            STAMP(5);
            asm volatile("s_waitcnt vmcnt(%c[n])" : "+v"(xr[0]), "+v"(xr[1]), "+v"(xr[2]), "+v"(xr[3]) : [n] "i"(((Y_SENT && POS_YF == 9) || DUMMY_N) ? 4 : 0) : "memory");
            final_landed<false>(fq);
#pragma unroll
            for (int i = 0; i < 4; ++i) *(LAS v4u*)(reg + CV_XR + 1024 * i + 16 * lane) = xr[i];
        }
        lf_arrive<F_CW>(fa);
        if (has) {
            const int d = task;
            STAMP(9); CSTAMP(15);
            const int pp = 1 + j + 8 * kq, rho = pp & 3;
            LAS char* abase = reg + rho * CV_COPY + 2 * (pp - rho);
            LAS char* bbase = reg + CV_XR + 4064 - 32 * j + 16 * kq - 3584;
            CvCtx cv;
#pragma unroll
            for (int m = 0; m < 8; ++m) cv_read<0>(cv, abase, bbase, m);
            {
                const int jj = 15 - j - 8 * kq;
                const unsigned val = (jj & 1) ? (dv << 16) : dv;
                v4u ad;
                ad.x = (jj >> 1) == 0 ? val : 0; ad.y = (jj >> 1) == 1 ? val : 0; ad.z = (jj >> 1) == 2 ? val : 0; ad.w = (jj >> 1) == 3 ? val : 0;
                if (jj < 0) ad = (v4u){0, 0, 0, 0};
                asm volatile("s_nop 1" : "+v"(ad));
#pragma unroll
                for (int c = 0; c < 8; ++c) asm volatile("v_mfma_f32_16x16x32_bf16 %0, %1, %2, 0" : "=&v"(cv.acc[c]) : "v"(ad), "v"(cv.B[0][c]));
            }
            if (CV_GATE) lf_wait_slow<F_HS>(fa, 4);
            __builtin_amdgcn_s_setprio(PRIO_C2);
            CSTAMP(16);
#pragma unroll 1
            for (int d2 = 0; d2 < 4; ++d2) {
                cv_half<0>(cv, abase + 64, bbase + 64, mkseq<36>::type{});
                if (CE_EARLY) lf_signal_if<F_CE>(fa, d2, 3);
                cv_half<1>(cv, abase + 128, bbase + 128, mkseq<36>::type{});
                abase += 128; bbase += 128;
            }
            asm volatile("s_nop 7\n\ts_nop 1" : "+v"(cv.acc[0]), "+v"(cv.acc[1]), "+v"(cv.acc[2]), "+v"(cv.acc[3]), "+v"(cv.acc[4]), "+v"(cv.acc[5]), "+v"(cv.acc[6]), "+v"(cv.acc[7]));
            f32x4 (&acc)[8] = cv.acc;
            CSTAMP(20);
            STAMP(10);
#pragma unroll
            for (int c = 0; c < 8; c += 2) {
                const auto x = __builtin_amdgcn_permlane16_swap(pk_bf16(acc[c][0], acc[c][1]), pk_bf16(acc[c + 1][0], acc[c + 1][1]), false, false);
                const auto y = __builtin_amdgcn_permlane16_swap(pk_bf16(acc[c][2], acc[c][3]), pk_bf16(acc[c + 1][2], acc[c + 1][3]), false, false);
                const v4u yv = {x[0], y[0], x[1], y[1]};
                __builtin_amdgcn_raw_buffer_store_b128(yv, rws, WS_Y + (d * S_ + 256 * (c + (kq & 1)) + 16 * j + 8 * (kq >> 1)) * 2, 0, YS_AUX);
            }
            lf_signal<F_YS>(fa);
            wait_vm<0>();
            publish(rws, WS_FY + d * FY_ST, tag);
            STAMP(11);
        }
        if (!has) { lf_signal<F_YS>(fa); if (CE_EARLY) lf_signal<F_CE>(fa); }
        __builtin_amdgcn_s_setprio(PRIO_F);
        final_prep(p, rws, fa, lds, fq, tag, R, ct, a, lane, nullptr STARG);
        wg_barrier();
        STFLUSH();
    }
}

extern "C" void kernel_launch(void* const* d_in, const int* in_sizes, int n_in, void* d_out, int out_size, void* d_ws, size_t ws_size, hipStream_t stream) {
    Params p;
    p.q = (const unsigned short*)d_in[0]; p.k = (const unsigned short*)d_in[1]; p.v = (const unsigned short*)d_in[2];
    p.shift = (const unsigned short*)d_in[3]; p.ssm = (const unsigned short*)d_in[4]; p.ssmD = (const unsigned short*)d_in[5];
    p.w1 = (const unsigned short*)d_in[7]; p.b1 = (const unsigned short*)d_in[8]; p.w2 = (const unsigned short*)d_in[9]; p.b2 = (const unsigned short*)d_in[10];
    p.gw = (const unsigned short*)d_in[11];
    p.out = (unsigned short*)d_out; p.ws = (char*)d_ws;
    (void)hipFuncSetAttribute((const void*)k_fused, hipFuncAttributeMaxDynamicSharedMemorySize, L_END);
    k_fused<<<256, 512, L_END, stream>>>(p);
}
```
